```python
import torch

def _softmax(x, axis=-1):
    return torch.softmax(x, dim=axis)
def _gelu(x):
    return torch.nn.functional.gelu(x, approximate="tanh")
def _silu(x):
    return torch.nn.functional.silu(x)
def _sigmoid(x):
    return torch.sigmoid(x)
def _layernorm(x, axis=-1, eps=1e-5):
    m = x.mean(dim=axis, keepdim=True)
    v = x.var(dim=axis, keepdim=True, unbiased=False)
    return (x - m) / torch.sqrt(v + eps)
def _scatter_add(tgt, idx, src, axis=0):
    return tgt.clone().index_add_(axis, idx, src)
def _segment_sum(src, seg, n):
    out = torch.zeros((n,) + src.shape[1:], dtype=src.dtype)
    return out.index_add_(0, seg, src)
def _pad(x, pw):
    flat = tuple(v for pair in reversed(pw) for v in pair)
    return torch.nn.functional.pad(x, flat)

kernel_name = 'standalone__gqa_softmax__near_ridge'

def setup_inputs():

    _w = (lambda a: (lambda t: t.to(torch.bfloat16) if t.is_floating_point() else t)(torch.as_tensor(a)))
    B, Hq, Hkv, Sq, Sk, D = 8, 16, 8, 128, 2048, 256
    return {
        "q": _w((lambda *s: torch.randn(*s, dtype=torch.bfloat16) * 0.8)(B, Hq, Sq, D)),
        "k": _w((lambda *s: torch.randn(*s, dtype=torch.bfloat16) * 0.8)(B, Hkv, Sk, D)),
        "v": _w((lambda *s: torch.randn(*s, dtype=torch.bfloat16) * 0.05)(B, Hkv, Sk, D)),
    }

def reference(q, k, v):
    B, Hq, Sq, D = q.shape
    Hkv = k.shape[1]
    rep = Hq // Hkv
    k = (lambda x, n, axis: torch.repeat_interleave(x, n, dim=axis))(k, rep, axis=1)
    v = (lambda x, n, axis: torch.repeat_interleave(x, n, dim=axis))(v, rep, axis=1)
    scale = D ** -0.5
    scores = torch.einsum("bhqd,bhkd->bhqk", q, k) * scale
    p = _softmax(scores, axis=-1)
    return torch.einsum("bhqk,bhkd->bhqd", p, v)
```

```cpp
#include <hip/hip_runtime.h>
#include <hip/hip_bf16.h>
constexpr int B=8,HQ=16,HKV=8,SQ=128,SK=2048,D=256;
__device__ __forceinline__ float bf2f(unsigned short x){ return __uint_as_float(((unsigned)x)<<16); }
__global__ __launch_bounds__(256) void naive(const unsigned short* __restrict__ q,const unsigned short* __restrict__ k,const unsigned short* __restrict__ v,__hip_bfloat16* __restrict__ o){
  constexpr int R=8;
  __shared__ float qs[R][D];
  __shared__ float ps[R][SK];
  __shared__ float red[R][4];
  int blk=blockIdx.x; int rg=blk%(SQ/R); int bh=blk/(SQ/R); int h=bh%HQ; int b=bh/HQ; int kvh=h/(HQ/HKV);
  int t=threadIdx.x;
  const unsigned short* qp=q+((size_t)(b*HQ+h)*SQ+rg*R)*D;
  for(int i=t;i<R*D;i+=256) qs[i/D][i%D]=bf2f(qp[i]);
  __syncthreads();
  const unsigned short* kp=k+(size_t)(b*HKV+kvh)*SK*D;
  const unsigned short* vp=v+(size_t)(b*HKV+kvh)*SK*D;
  for(int j=t;j<SK;j+=256){
    float acc[R]; for(int r=0;r<R;r++) acc[r]=0;
    for(int d=0;d<D;d+=8){
      uint4 kk=*(const uint4*)(kp+(size_t)j*D+d);
      unsigned w[4]={kk.x,kk.y,kk.z,kk.w};
      for(int e=0;e<4;e++){ float k0=__uint_as_float(w[e]<<16),k1=__uint_as_float(w[e]&0xffff0000u);
        for(int r=0;r<R;r++) acc[r]+=qs[r][d+2*e]*k0+qs[r][d+2*e+1]*k1; }
    }
    for(int r=0;r<R;r++) ps[r][j]=acc[r]*0.0625f;
  }
  __syncthreads();
  for(int r=0;r<R;r++){
    float m=-INFINITY; for(int j=t;j<SK;j+=256) m=fmaxf(m,ps[r][j]);
    for(int s=32;s>0;s>>=1) m=fmaxf(m,__shfl_xor(m,s));
    if((t&63)==0) red[r][t>>6]=m;
    __syncthreads();
    m=fmaxf(fmaxf(red[r][0],red[r][1]),fmaxf(red[r][2],red[r][3]));
    __syncthreads();
    float l=0; for(int j=t;j<SK;j+=256){ float p=__expf(ps[r][j]-m); ps[r][j]=p; l+=p; }
    for(int s=32;s>0;s>>=1) l+=__shfl_xor(l,s);
    if((t&63)==0) red[r][t>>6]=l;
    __syncthreads();
  }
  float acc[R]; for(int r=0;r<R;r++) acc[r]=0;
  for(int j=0;j<SK;j++){ float vv=bf2f(vp[(size_t)j*D+t]); for(int r=0;r<R;r++) acc[r]+=ps[r][j]*vv; }
  for(int r=0;r<R;r++){ float l=red[r][0]+red[r][1]+red[r][2]+red[r][3]; o[((size_t)(b*HQ+h)*SQ+rg*R+r)*D+t]=__float2bfloat16(acc[r]/l); }
}
extern "C" void kernel_launch(void* const* d_in, const int* in_sizes, int n_in, void* d_out, int out_size, void* d_ws, size_t ws_size, hipStream_t stream) {
  naive<<<B*HQ*SQ/8,256,0,stream>>>((const unsigned short*)d_in[0],(const unsigned short*)d_in[1],(const unsigned short*)d_in[2],(__hip_bfloat16*)d_out);
}
```

```cpp
#include <hip/hip_runtime.h>
#include <hip/hip_bf16.h>
constexpr int B=8,HQ=16,HKV=8,SQ=128,SK=2048,D=256;
__device__ __forceinline__ float bf2f(unsigned short x){ return __uint_as_float(((unsigned)x)<<16); }
__global__ __launch_bounds__(256) void naive(const unsigned short* __restrict__ q,const unsigned short* __restrict__ k,const unsigned short* __restrict__ v,__hip_bfloat16* __restrict__ o){
  constexpr int R=8;
  __shared__ float qs[R][D];
  __shared__ float ps[R][SK];
  __shared__ float red[R][4];
  int blk=blockIdx.x; int rg=blk%(SQ/R); int bh=blk/(SQ/R); int h=bh%HQ; int b=bh/HQ; int kvh=h/(HQ/HKV);
  int t=threadIdx.x;
  const unsigned short* qp=q+((size_t)(b*HQ+h)*SQ+rg*R)*D;
  for(int i=t;i<R*D;i+=256) qs[i/D][i%D]=bf2f(qp[i]);
  __syncthreads();
  const unsigned short* kp=k+(size_t)(b*HKV+kvh)*SK*D;
  const unsigned short* vp=v+(size_t)(b*HKV+kvh)*SK*D;
  for(int j=t;j<SK;j+=256){
    float acc[R]; for(int r=0;r<R;r++) acc[r]=0;
    for(int d=0;d<D;d+=8){
      uint4 kk=*(const uint4*)(kp+(size_t)j*D+d);
      unsigned w[4]={kk.x,kk.y,kk.z,kk.w};
      for(int e=0;e<4;e++){ float k0=__uint_as_float(w[e]<<16),k1=__uint_as_float(w[e]&0xffff0000u);
        for(int r=0;r<R;r++) acc[r]+=qs[r][d+2*e]*k0+qs[r][d+2*e+1]*k1; }
    }
    for(int r=0;r<R;r++) ps[r][j]=acc[r]*0.0625f;
  }
  __syncthreads();
  for(int r=0;r<R;r++){
    float m=-INFINITY; for(int j=t;j<SK;j+=256) m=fmaxf(m,ps[r][j]);
    for(int s=32;s>0;s>>=1) m=fmaxf(m,__shfl_xor(m,s));
    if((t&63)==0) red[r][t>>6]=m;
    __syncthreads();
    m=fmaxf(fmaxf(red[r][0],red[r][1]),fmaxf(red[r][2],red[r][3]));
    __syncthreads();
    float l=0; for(int j=t;j<SK;j+=256){ float p=__expf(ps[r][j]-m); ps[r][j]=p; l+=p; }
    for(int s=32;s>0;s>>=1) l+=__shfl_xor(l,s);
    if((t&63)==0) red[r][t>>6]=l;
    __syncthreads();
  }
  float acc[R]; for(int r=0;r<R;r++) acc[r]=0;
  for(int j=0;j<SK;j++){ float vv=bf2f(vp[(size_t)j*D+t]); for(int r=0;r<R;r++) acc[r]+=ps[r][j]*vv; }
  for(int r=0;r<R;r++){ float l=red[r][0]+red[r][1]+red[r][2]+red[r][3]; o[((size_t)(b*HQ+h)*SQ+rg*R+r)*D+t]=__float2bfloat16(acc[r]/l); }
}

typedef float f32x16 __attribute__((ext_vector_type(16)));
typedef short bf16x8 __attribute__((ext_vector_type(8)));
typedef unsigned u32x4 __attribute__((ext_vector_type(4)));
template<int SHARE,int WIN>
__global__ __launch_bounds__(256,1) void probe(const char* __restrict__ K,const char* __restrict__ V,unsigned long long* stamps){
  extern __shared__ __attribute__((aligned(16))) char lds[];
  const int tid=threadIdx.x, lane=tid&63; const int w=__builtin_amdgcn_readfirstlane(tid>>6);
  const int blk=blockIdx.x; const int xcd=blk&7, idx=blk>>3;
  const int pair=xcd*8+(idx>>2); const int mem=idx&3;
  unsigned long long t0=__builtin_amdgcn_s_memrealtime();
  constexpr int CUB=(1<<20)/4*SHARE;
  const int part = SHARE==4?0:(SHARE==2?(mem>>1):mem);
  const char* kb=K+(size_t)pair*(1<<20)+(size_t)part*CUB; const char* vb=V+(size_t)pair*(1<<20)+(size_t)part*CUB;
  constexpr int NP=CUB*2/4/1024;
  constexpr int RING=36;
  auto srdK=__builtin_amdgcn_make_buffer_rsrc((void*)kb,(short)0,CUB,0x00020000);
  auto srdV=__builtin_amdgcn_make_buffer_rsrc((void*)vb,(short)0,CUB,0x00020000);
  unsigned ldsb=(unsigned)(size_t)lds + w*RING*1024;
  unsigned voff=lane*16;
  int slot=0;
  for(int p=0;p<NP;p+=8){
    #pragma unroll
    for(int j=0;j<8;j++){
      unsigned soff=((p>>3)*4+w)*4096+(j&3)*1024;
      unsigned m0=ldsb+slot*1024; slot++; if(slot==RING) slot=0;
      asm volatile("s_waitcnt vmcnt(%0)"::"i"(WIN-1):"memory");
      if(j<4) asm volatile("s_mov_b32 m0, %0\n\ts_nop 0\n\tbuffer_load_dwordx4 %1, %2, %3 offen nt lds"::"s"(m0),"v"(voff),"s"(srdK),"s"(soff):"memory");
      else    asm volatile("s_mov_b32 m0, %0\n\ts_nop 0\n\tbuffer_load_dwordx4 %1, %2, %3 offen nt lds"::"s"(m0),"v"(voff),"s"(srdV),"s"(soff):"memory");
    }
  }
  asm volatile("s_waitcnt vmcnt(0) lgkmcnt(0)":::"memory");
  unsigned long long t1=__builtin_amdgcn_s_memrealtime();
  if(lane==0){ stamps[(blk*4+w)*2+0]=t0; stamps[(blk*4+w)*2+1]=t1; }
}
__global__ void spin(const unsigned long long* stamps){
  unsigned long long mn=~0ull,mx=0;
  for(int i=threadIdx.x;i<1024;i+=64){ unsigned long long a=stamps[i*2],b=stamps[i*2+1]; mn=a<mn?a:mn; mx=b>mx?b:mx; }
  for(int s=32;s>0;s>>=1){ unsigned long long a=__shfl_xor(mn,s),b=__shfl_xor(mx,s); mn=a<mn?a:mn; mx=b>mx?b:mx; }
  unsigned long long d=mx-mn; if(d>10000) d=10000;
  unsigned long long t0=__builtin_amdgcn_s_memrealtime();
  while(__builtin_amdgcn_s_memrealtime()-t0 < d*100) __builtin_amdgcn_s_sleep(8);
}
extern "C" void kernel_launch(void* const* d_in, const int* in_sizes, int n_in, void* d_out, int out_size, void* d_ws, size_t ws_size, hipStream_t stream) {
  hipFuncSetAttribute((void*)probe<1,16>,hipFuncAttributeMaxDynamicSharedMemorySize,163840);
  probe<1,16><<<256,256,163840,stream>>>((const char*)d_in[1],(const char*)d_in[2],(unsigned long long*)d_ws);
  spin<<<1,64,0,stream>>>((const unsigned long long*)d_ws);
  naive<<<B*HQ*SQ/8,256,0,stream>>>((const unsigned short*)d_in[0],(const unsigned short*)d_in[1],(const unsigned short*)d_in[2],(__hip_bfloat16*)d_out);
}
```

```cpp
#include <hip/hip_runtime.h>
#include <hip/hip_bf16.h>

__global__ __launch_bounds__(256, 1) void gqa_attn_kernel(const void* q, const void* k, const void* v, void* out, void* ws) {
  const unsigned wave = __builtin_amdgcn_readfirstlane(threadIdx.x >> 6);
  const unsigned blk = blockIdx.x;
  asm volatile(
    "s_load_dwordx8 s[84:91], %0, 0x0\n"
    "s_load_dwordx2 s[92:93], %0, 0x20\n"
    "s_getpc_b64 s[50:51]\n"
    ".Lpc_%=:\n"
    "v_mbcnt_lo_u32_b32 v225, -1, 0\n"
    "v_mbcnt_hi_u32_b32 v225, -1, v225\n"
    "s_lshl_b32 s53, %2, 13\n"
    "v_lshl_add_u32 v251, v225, 7, s53\n"
    "v_min_u32 v251, .Lend_%=-.Lpc_%=, v251\n"
    "global_load_dword v251, v251, s[50:51]\n"
    "s_and_b32 s53, %1, 7\n"
    "s_lshr_b32 s54, %1, 5\n"
    "s_lshl3_add_u32 s53, s53, s54\n"
    "s_bfe_u32 s52, %1, 65540\n"
    "s_bfe_u32 s54, %1, 0x10003\n"
    "s_mov_b32 s49, %2\n"
    "s_lshl_b32 s55, s53, 1\n"
    "s_add_u32 s55, s55, s52\n"
    "s_lshl_b32 s55, s55, 19\n"
    "s_lshl1_add_u32 s56, s53, s54\n"
    "s_lshl2_add_u32 s56, s56, %2\n"
    "s_lshl_b32 s56, s56, 14\n"
    "s_mov_b32 s22, 16384\n"
    "s_mov_b32 s23, 0x20000\n"
    "v_lshrrev_b32 v232, 5, v225\n"
    "v_xor_b32 v234, v232, v225\n"
    "v_lshlrev_b32 v238, 4, v234\n"
    "v_xor_b32 v239, 32, v238\n"
    "v_xor_b32 v240, 64, v238\n"
    "v_xor_b32 v241, 96, v238\n"
    "v_xor_b32 v242, 128, v238\n"
    "v_xor_b32 v243, 160, v238\n"
    "v_xor_b32 v244, 192, v238\n"
    "v_xor_b32 v245, 224, v238\n"
    "v_and_b32 v233, 31, v225\n"
    "v_lshlrev_b32 v235, 9, v232\n"
    "s_lshl_b32 s45, s49, 12\n"
    "s_mov_b32 s44, 0\n"
    "s_waitcnt lgkmcnt(0)\n"
    "s_add_u32 s20, s84, s56\n"
    "s_addc_u32 s21, s85, 0\n"
    "s_and_b32 s21, s21, 0xffff\n"
    "buffer_load_dwordx4 v[128:131], v238, s[20:23], s44 offen offset:0 nt\n"
    "s_load_dword s95, s[86:87], s55\n"
    "s_load_dword s95, s[88:89], s55\n"
    "s_load_dword s95, s[90:91], s56\n"
    "buffer_load_dwordx4 v[132:135], v239, s[20:23], s44 offen offset:1024 nt\n"
    "buffer_load_dwordx4 v[136:139], v240, s[20:23], s44 offen offset:2048 nt\n"
    "s_add_u32 s24, s86, s55\n"
    "s_addc_u32 s25, s87, 0\n"
    "s_and_b32 s25, s25, 0xffff\n"
    "s_mov_b32 s26, 524288\n"
    "s_mov_b32 s27, 0x20000\n"
    "s_lshl_b32 s53, s49, 7\n"
    "s_and_b32 s53, s53, 128\n"
    "s_or_b32 s53, s53, s45\n"
    "v_xor_b32 v212, s53, v238\n"
    "v_xor_b32 v213, s53, v239\n"
    "v_xor_b32 v214, s53, v240\n"
    "v_xor_b32 v215, s53, v241\n"
    "s_add_u32 s28, s88, s55\n"
    "s_addc_u32 s29, s89, 0\n"
    "s_sub_u32 s28, s28, 16384\n"
    "s_subb_u32 s29, s29, 0\n"
    "s_and_b32 s29, s29, 0xffff\n"
    "s_mov_b32 s30, 540672\n"
    "s_mov_b32 s31, 0x20000\n"
    "s_add_u32 s16, s90, s56\n"
    "s_addc_u32 s17, s91, 0\n"
    "s_and_b32 s17, s17, 0xffff\n"
    "s_mov_b32 s18, 16384\n"
    "s_mov_b32 s19, 0x20000\n"
    "s_xor_b32 s55, %1, 0\n"
    "buffer_load_dwordx4 v[140:143], v241, s[20:23], s44 offen offset:3072 nt\n"
    "s_lshl_b32 s55, s55, 2\n"
    "s_add_u32 s55, s55, s49\n"
    "s_mul_i32 s55, s55, 10240\n"
    "s_load_dword s95, s[92:93], s55\n"
    "s_add_u32 s36, s92, s55\n"
    "s_addc_u32 s37, s93, 0\n"
    "s_and_b32 s37, s37, 0xffff\n"
    "s_mov_b32 s38, 10240\n"
    "s_mov_b32 s39, 0x20000\n"
    "s_xor_b32 s55, %1, 16\n"
    "s_lshl_b32 s55, s55, 2\n"
    "s_add_u32 s55, s55, s49\n"
    "s_mul_i32 s55, s55, 10240\n"
    "s_load_dword s95, s[92:93], s55\n"
    "s_add_u32 s40, s92, s55\n"
    "s_addc_u32 s41, s93, 0\n"
    "s_and_b32 s41, s41, 0xffff\n"
    "s_mov_b32 s42, 10240\n"
    "s_mov_b32 s43, 0x20000\n"
    "v_and_b32 v226, 15, v225\n"
    "s_mov_b32 s44, 4096\n"
    "buffer_load_dwordx4 v[144:147], v242, s[20:23], s44 offen offset:0 nt\n"
    "v_lshl_or_b32 v226, v232, 4, v226\n"
    "v_lshlrev_b32 v226, 4, v226\n"
    "v_lshlrev_b32 v228, 16, v225\n"
    "v_lshrrev_b32 v229, 4, v226\n"
    "s_mov_b32 s58, 8192\n"
    "s_mov_b32 s46, 0x3db8aa3b\n"
    "s_mov_b32 s47, 0xbf800000\n"
    "s_mov_b32 s48, 6\n"
    "s_lshl2_add_u32 s57, s54, s49\n"
    "v_lshl_add_u32 v230, s57, 6, v225\n"
    "v_mul_u32_u24 v227, 16, v230\n"
    "v_mul_u32_u24 v230, 16, v230\n"
    "buffer_load_dwordx4 v[148:151], v243, s[20:23], s44 offen offset:1024 nt\n"
    "s_mov_b32 s57, 491520\n"
    "s_mov_b32 s100, 499712\n"
    "s_getreg_b32 s81, hwreg(HW_REG_XCC_ID, 0, 3)\n"
    "s_lshr_b32 s81, 0xaa, s81\n"
    "s_and_b32 s81, s81, 1\n"
    "s_mul_i32 s81, s81, 0xff0edb98\n"
    "s_add_u32 s80, s81, 0x101369c\n"
    "s_mov_b32 s81, 0x0\n"
    "s_xor_b32 s55, s52, 1\n"
    "s_lshl_b32 s55, s55, 4\n"
    "v_lshlrev_b32 v234, 1, v232\n"
    "v_xor_b32 v234, v234, v233\n"
    "buffer_load_dwordx4 v[152:155], v244, s[20:23], s44 offen offset:2048 nt\n"
    "v_xor_b32 v234, s55, v234\n"
    "v_add_u32 v235, s45, v235\n"
    "v_lshl_add_u32 v216, v234, 4, v235\n"
    "v_xor_b32 v217, 64, v216\n"
    "v_xor_b32 v218, 128, v216\n"
    "v_xor_b32 v219, 192, v216\n"
    "v_lshrrev_b32 v236, 4, v225\n"
    "v_and_b32 v237, 15, v225\n"
    "v_xor_b32 v234, v236, v237\n"
    "v_lshlrev_b32 v235, 9, v237\n"
    "v_lshl_add_u32 v200, v234, 4, v235\n"
    "v_xor_b32 v201, 64, v200\n"
    "buffer_load_dwordx4 v[156:159], v245, s[20:23], s44 offen offset:3072 nt\n"
    "v_xor_b32 v202, 128, v200\n"
    "v_xor_b32 v203, 192, v200\n"
    "s_lshl_b32 s54, s49, 14\n"
    "s_add_u32 s54, s54, 98304\n"
    "v_add_u32 v246, s54, v200\n"
    "v_add_u32 v247, s54, v201\n"
    "v_add_u32 v248, s54, v202\n"
    "v_add_u32 v249, s54, v203\n"
    "v_lshl_add_u32 v250, v225, 4, s54\n"
    "v_lshrrev_b32 v234, 2, v225\n"
    "v_and_b32 v235, 7, v234\n"
    "v_lshlrev_b32 v234, 9, v234\n"
    "s_mov_b32 s44, 8192\n"
    "buffer_load_dwordx4 v[160:163], v238, s[20:23], s44 offen offset:0 nt\n"
    "v_lshl_add_u32 v234, v235, 5, v234\n"
    "v_and_b32 v235, 3, v225\n"
    "v_lshl_add_u32 v234, v235, 3, v234\n"
    "v_add_u32 v204, 0x10000, v234\n"
    "v_xor_b32 v205, 32, v204\n"
    "v_xor_b32 v206, 64, v204\n"
    "v_xor_b32 v207, 96, v204\n"
    "v_xor_b32 v208, 128, v204\n"
    "v_xor_b32 v209, 160, v204\n"
    "v_xor_b32 v210, 192, v204\n"
    "v_xor_b32 v211, 224, v204\n"
    "v_mov_b32 v252, 0\n"
    "buffer_load_dwordx4 v[164:167], v239, s[20:23], s44 offen offset:1024 nt\n"
    "v_mov_b32 v253, 0\n"
    "v_mov_b32 v254, 0\n"
    "v_mov_b32 v255, 0\n"
    "s_nop 1\n"
    "v_mfma_f32_32x32x16_bf16 v[0:15], v[252:255], v[252:255], 0\n"
    "v_mfma_f32_32x32x16_bf16 v[16:31], v[252:255], v[252:255], 0\n"
    "v_mfma_f32_32x32x16_bf16 v[32:47], v[252:255], v[252:255], 0\n"
    "v_mfma_f32_32x32x16_bf16 v[48:63], v[252:255], v[252:255], 0\n"
    "v_mfma_f32_32x32x16_bf16 v[64:79], v[252:255], v[252:255], 0\n"
    "v_mfma_f32_32x32x16_bf16 v[80:95], v[252:255], v[252:255], 0\n"
    "v_mfma_f32_32x32x16_bf16 v[96:111], v[252:255], v[252:255], 0\n"
    "v_mfma_f32_32x32x16_bf16 v[112:127], v[252:255], v[252:255], 0\n"
    "s_waitcnt vmcnt(9)\n"
    "buffer_load_dwordx4 v[168:171], v240, s[20:23], s44 offen offset:2048 nt\n"
    "buffer_load_dwordx4 v[172:175], v241, s[20:23], s44 offen offset:3072 nt\n"
    "v_lshlrev_b32 v252, 16, v128\n"
    "v_and_b32 v253, 0xffff0000, v128\n"
    "v_lshlrev_b32 v254, 16, v129\n"
    "v_and_b32 v255, 0xffff0000, v129\n"
    "v_pk_mul_f32 v[252:253], v[252:253], s[46:47] op_sel_hi:[1,0]\n"
    "v_pk_mul_f32 v[254:255], v[254:255], s[46:47] op_sel_hi:[1,0]\n"
    "v_cvt_pk_bf16_f32 v128, v252, v253\n"
    "v_cvt_pk_bf16_f32 v129, v254, v255\n"
    "v_lshlrev_b32 v252, 16, v130\n"
    "v_and_b32 v253, 0xffff0000, v130\n"
    "v_lshlrev_b32 v254, 16, v131\n"
    "v_and_b32 v255, 0xffff0000, v131\n"
    "v_pk_mul_f32 v[252:253], v[252:253], s[46:47] op_sel_hi:[1,0]\n"
    "v_pk_mul_f32 v[254:255], v[254:255], s[46:47] op_sel_hi:[1,0]\n"
    "v_cvt_pk_bf16_f32 v130, v252, v253\n"
    "v_cvt_pk_bf16_f32 v131, v254, v255\n"
    "ds_write_b128 v250, v[128:131] offset:0\n"
    "s_waitcnt vmcnt(10)\n"
    "s_mov_b32 s44, 12288\n"
    "buffer_load_dwordx4 v[176:179], v242, s[20:23], s44 offen offset:0 nt\n"
    "buffer_load_dwordx4 v[180:183], v243, s[20:23], s44 offen offset:1024 nt\n"
    "v_lshlrev_b32 v252, 16, v132\n"
    "v_and_b32 v253, 0xffff0000, v132\n"
    "v_lshlrev_b32 v254, 16, v133\n"
    "v_and_b32 v255, 0xffff0000, v133\n"
    "v_pk_mul_f32 v[252:253], v[252:253], s[46:47] op_sel_hi:[1,0]\n"
    "v_pk_mul_f32 v[254:255], v[254:255], s[46:47] op_sel_hi:[1,0]\n"
    "v_cvt_pk_bf16_f32 v132, v252, v253\n"
    "v_cvt_pk_bf16_f32 v133, v254, v255\n"
    "v_lshlrev_b32 v252, 16, v134\n"
    "v_and_b32 v253, 0xffff0000, v134\n"
    "v_lshlrev_b32 v254, 16, v135\n"
    "v_and_b32 v255, 0xffff0000, v135\n"
    "v_pk_mul_f32 v[252:253], v[252:253], s[46:47] op_sel_hi:[1,0]\n"
    "v_pk_mul_f32 v[254:255], v[254:255], s[46:47] op_sel_hi:[1,0]\n"
    "v_cvt_pk_bf16_f32 v134, v252, v253\n"
    "v_cvt_pk_bf16_f32 v135, v254, v255\n"
    "ds_write_b128 v250, v[132:135] offset:1024\n"
    "s_waitcnt vmcnt(11)\n"
    "buffer_load_dwordx4 v[184:187], v244, s[20:23], s44 offen offset:2048 nt\n"
    "buffer_load_dwordx4 v[188:191], v245, s[20:23], s44 offen offset:3072 nt\n"
    "v_lshlrev_b32 v252, 16, v136\n"
    "v_and_b32 v253, 0xffff0000, v136\n"
    "v_lshlrev_b32 v254, 16, v137\n"
    "v_and_b32 v255, 0xffff0000, v137\n"
    "v_pk_mul_f32 v[252:253], v[252:253], s[46:47] op_sel_hi:[1,0]\n"
    "v_pk_mul_f32 v[254:255], v[254:255], s[46:47] op_sel_hi:[1,0]\n"
    "v_cvt_pk_bf16_f32 v136, v252, v253\n"
    "v_cvt_pk_bf16_f32 v137, v254, v255\n"
    "v_lshlrev_b32 v252, 16, v138\n"
    "v_and_b32 v253, 0xffff0000, v138\n"
    "v_lshlrev_b32 v254, 16, v139\n"
    "v_and_b32 v255, 0xffff0000, v139\n"
    "v_pk_mul_f32 v[252:253], v[252:253], s[46:47] op_sel_hi:[1,0]\n"
    "v_pk_mul_f32 v[254:255], v[254:255], s[46:47] op_sel_hi:[1,0]\n"
    "v_cvt_pk_bf16_f32 v138, v252, v253\n"
    "v_cvt_pk_bf16_f32 v139, v254, v255\n"
    "ds_write_b128 v250, v[136:139] offset:2048\n"
    "s_waitcnt vmcnt(12)\n"
    "s_mov_b32 s44, 0\n"
    "s_add_u32 m0, s45, 0\n"
    "s_nop 0\n"
    "buffer_load_dwordx4 v212, s[24:27], s44 offen offset:0 nt lds\n"
    "v_lshlrev_b32 v252, 16, v140\n"
    "v_and_b32 v253, 0xffff0000, v140\n"
    "v_lshlrev_b32 v254, 16, v141\n"
    "v_and_b32 v255, 0xffff0000, v141\n"
    "v_pk_mul_f32 v[252:253], v[252:253], s[46:47] op_sel_hi:[1,0]\n"
    "v_pk_mul_f32 v[254:255], v[254:255], s[46:47] op_sel_hi:[1,0]\n"
    "v_cvt_pk_bf16_f32 v140, v252, v253\n"
    "v_cvt_pk_bf16_f32 v141, v254, v255\n"
    "v_lshlrev_b32 v252, 16, v142\n"
    "v_and_b32 v253, 0xffff0000, v142\n"
    "v_lshlrev_b32 v254, 16, v143\n"
    "v_and_b32 v255, 0xffff0000, v143\n"
    "v_pk_mul_f32 v[252:253], v[252:253], s[46:47] op_sel_hi:[1,0]\n"
    "v_pk_mul_f32 v[254:255], v[254:255], s[46:47] op_sel_hi:[1,0]\n"
    "v_cvt_pk_bf16_f32 v142, v252, v253\n"
    "v_cvt_pk_bf16_f32 v143, v254, v255\n"
    "ds_write_b128 v250, v[140:143] offset:3072\n"
    "s_waitcnt vmcnt(12)\n"
    "buffer_load_dwordx4 v213, s[24:27], s44 offen offset:1024 nt lds\n"
    "v_lshlrev_b32 v252, 16, v144\n"
    "v_and_b32 v253, 0xffff0000, v144\n"
    "v_lshlrev_b32 v254, 16, v145\n"
    "v_and_b32 v255, 0xffff0000, v145\n"
    "v_pk_mul_f32 v[252:253], v[252:253], s[46:47] op_sel_hi:[1,0]\n"
    "v_pk_mul_f32 v[254:255], v[254:255], s[46:47] op_sel_hi:[1,0]\n"
    "v_cvt_pk_bf16_f32 v144, v252, v253\n"
    "v_cvt_pk_bf16_f32 v145, v254, v255\n"
    "v_lshlrev_b32 v252, 16, v146\n"
    "v_and_b32 v253, 0xffff0000, v146\n"
    "v_lshlrev_b32 v254, 16, v147\n"
    "v_and_b32 v255, 0xffff0000, v147\n"
    "v_pk_mul_f32 v[252:253], v[252:253], s[46:47] op_sel_hi:[1,0]\n"
    "v_pk_mul_f32 v[254:255], v[254:255], s[46:47] op_sel_hi:[1,0]\n"
    "v_cvt_pk_bf16_f32 v146, v252, v253\n"
    "v_cvt_pk_bf16_f32 v147, v254, v255\n"
    "ds_write_b128 v250, v[144:147] offset:4096\n"
    "s_waitcnt vmcnt(12)\n"
    "buffer_load_dwordx4 v214, s[24:27], s44 offen offset:2048 nt lds\n"
    "v_lshlrev_b32 v252, 16, v148\n"
    "v_and_b32 v253, 0xffff0000, v148\n"
    "v_lshlrev_b32 v254, 16, v149\n"
    "v_and_b32 v255, 0xffff0000, v149\n"
    "v_pk_mul_f32 v[252:253], v[252:253], s[46:47] op_sel_hi:[1,0]\n"
    "v_pk_mul_f32 v[254:255], v[254:255], s[46:47] op_sel_hi:[1,0]\n"
    "v_cvt_pk_bf16_f32 v148, v252, v253\n"
    "v_cvt_pk_bf16_f32 v149, v254, v255\n"
    "v_lshlrev_b32 v252, 16, v150\n"
    "v_and_b32 v253, 0xffff0000, v150\n"
    "v_lshlrev_b32 v254, 16, v151\n"
    "v_and_b32 v255, 0xffff0000, v151\n"
    "v_pk_mul_f32 v[252:253], v[252:253], s[46:47] op_sel_hi:[1,0]\n"
    "v_pk_mul_f32 v[254:255], v[254:255], s[46:47] op_sel_hi:[1,0]\n"
    "v_cvt_pk_bf16_f32 v150, v252, v253\n"
    "v_cvt_pk_bf16_f32 v151, v254, v255\n"
    "ds_write_b128 v250, v[148:151] offset:5120\n"
    "s_waitcnt vmcnt(12)\n"
    "buffer_load_dwordx4 v215, s[24:27], s44 offen offset:3072 nt lds\n"
    "v_lshlrev_b32 v252, 16, v152\n"
    "v_and_b32 v253, 0xffff0000, v152\n"
    "v_lshlrev_b32 v254, 16, v153\n"
    "v_and_b32 v255, 0xffff0000, v153\n"
    "v_pk_mul_f32 v[252:253], v[252:253], s[46:47] op_sel_hi:[1,0]\n"
    "v_pk_mul_f32 v[254:255], v[254:255], s[46:47] op_sel_hi:[1,0]\n"
    "v_cvt_pk_bf16_f32 v152, v252, v253\n"
    "v_cvt_pk_bf16_f32 v153, v254, v255\n"
    "v_lshlrev_b32 v252, 16, v154\n"
    "v_and_b32 v253, 0xffff0000, v154\n"
    "v_lshlrev_b32 v254, 16, v155\n"
    "v_and_b32 v255, 0xffff0000, v155\n"
    "v_pk_mul_f32 v[252:253], v[252:253], s[46:47] op_sel_hi:[1,0]\n"
    "v_pk_mul_f32 v[254:255], v[254:255], s[46:47] op_sel_hi:[1,0]\n"
    "v_cvt_pk_bf16_f32 v154, v252, v253\n"
    "v_cvt_pk_bf16_f32 v155, v254, v255\n"
    "ds_write_b128 v250, v[152:155] offset:6144\n"
    "s_waitcnt vmcnt(12)\n"
    "s_mov_b32 s44, 16384\n"
    "s_add_u32 m0, s45, 16384\n"
    "s_nop 0\n"
    "buffer_load_dwordx4 v212, s[24:27], s44 offen offset:0 nt lds\n"
    "v_lshlrev_b32 v252, 16, v156\n"
    "v_and_b32 v253, 0xffff0000, v156\n"
    "v_lshlrev_b32 v254, 16, v157\n"
    "v_and_b32 v255, 0xffff0000, v157\n"
    "v_pk_mul_f32 v[252:253], v[252:253], s[46:47] op_sel_hi:[1,0]\n"
    "v_pk_mul_f32 v[254:255], v[254:255], s[46:47] op_sel_hi:[1,0]\n"
    "v_cvt_pk_bf16_f32 v156, v252, v253\n"
    "v_cvt_pk_bf16_f32 v157, v254, v255\n"
    "v_lshlrev_b32 v252, 16, v158\n"
    "v_and_b32 v253, 0xffff0000, v158\n"
    "v_lshlrev_b32 v254, 16, v159\n"
    "v_and_b32 v255, 0xffff0000, v159\n"
    "v_pk_mul_f32 v[252:253], v[252:253], s[46:47] op_sel_hi:[1,0]\n"
    "v_pk_mul_f32 v[254:255], v[254:255], s[46:47] op_sel_hi:[1,0]\n"
    "v_cvt_pk_bf16_f32 v158, v252, v253\n"
    "v_cvt_pk_bf16_f32 v159, v254, v255\n"
    "ds_write_b128 v250, v[156:159] offset:7168\n"
    "s_waitcnt vmcnt(12)\n"
    "buffer_load_dwordx4 v213, s[24:27], s44 offen offset:1024 nt lds\n"
    "v_lshlrev_b32 v252, 16, v160\n"
    "v_and_b32 v253, 0xffff0000, v160\n"
    "v_lshlrev_b32 v254, 16, v161\n"
    "v_and_b32 v255, 0xffff0000, v161\n"
    "v_pk_mul_f32 v[252:253], v[252:253], s[46:47] op_sel_hi:[1,0]\n"
    "v_pk_mul_f32 v[254:255], v[254:255], s[46:47] op_sel_hi:[1,0]\n"
    "v_cvt_pk_bf16_f32 v160, v252, v253\n"
    "v_cvt_pk_bf16_f32 v161, v254, v255\n"
    "v_lshlrev_b32 v252, 16, v162\n"
    "v_and_b32 v253, 0xffff0000, v162\n"
    "v_lshlrev_b32 v254, 16, v163\n"
    "v_and_b32 v255, 0xffff0000, v163\n"
    "v_pk_mul_f32 v[252:253], v[252:253], s[46:47] op_sel_hi:[1,0]\n"
    "v_pk_mul_f32 v[254:255], v[254:255], s[46:47] op_sel_hi:[1,0]\n"
    "v_cvt_pk_bf16_f32 v162, v252, v253\n"
    "v_cvt_pk_bf16_f32 v163, v254, v255\n"
    "ds_write_b128 v250, v[160:163] offset:8192\n"
    "ds_read_b128 a[0:3], v246 offset:0\n"
    "s_waitcnt vmcnt(12)\n"
    "buffer_load_dwordx4 v214, s[24:27], s44 offen offset:2048 nt lds\n"
    "v_lshlrev_b32 v252, 16, v164\n"
    "v_and_b32 v253, 0xffff0000, v164\n"
    "v_lshlrev_b32 v254, 16, v165\n"
    "v_and_b32 v255, 0xffff0000, v165\n"
    "v_pk_mul_f32 v[252:253], v[252:253], s[46:47] op_sel_hi:[1,0]\n"
    "v_pk_mul_f32 v[254:255], v[254:255], s[46:47] op_sel_hi:[1,0]\n"
    "v_cvt_pk_bf16_f32 v164, v252, v253\n"
    "v_cvt_pk_bf16_f32 v165, v254, v255\n"
    "v_lshlrev_b32 v252, 16, v166\n"
    "v_and_b32 v253, 0xffff0000, v166\n"
    "v_lshlrev_b32 v254, 16, v167\n"
    "v_and_b32 v255, 0xffff0000, v167\n"
    "v_pk_mul_f32 v[252:253], v[252:253], s[46:47] op_sel_hi:[1,0]\n"
    "v_pk_mul_f32 v[254:255], v[254:255], s[46:47] op_sel_hi:[1,0]\n"
    "v_cvt_pk_bf16_f32 v166, v252, v253\n"
    "v_cvt_pk_bf16_f32 v167, v254, v255\n"
    "ds_write_b128 v250, v[164:167] offset:9216\n"
    "ds_read_b128 a[4:7], v247 offset:0\n"
    "s_waitcnt vmcnt(12)\n"
    "buffer_load_dwordx4 v215, s[24:27], s44 offen offset:3072 nt lds\n"
    "v_lshlrev_b32 v252, 16, v168\n"
    "v_and_b32 v253, 0xffff0000, v168\n"
    "v_lshlrev_b32 v254, 16, v169\n"
    "v_and_b32 v255, 0xffff0000, v169\n"
    "v_pk_mul_f32 v[252:253], v[252:253], s[46:47] op_sel_hi:[1,0]\n"
    "v_pk_mul_f32 v[254:255], v[254:255], s[46:47] op_sel_hi:[1,0]\n"
    "v_cvt_pk_bf16_f32 v168, v252, v253\n"
    "v_cvt_pk_bf16_f32 v169, v254, v255\n"
    "v_lshlrev_b32 v252, 16, v170\n"
    "v_and_b32 v253, 0xffff0000, v170\n"
    "v_lshlrev_b32 v254, 16, v171\n"
    "v_and_b32 v255, 0xffff0000, v171\n"
    "v_pk_mul_f32 v[252:253], v[252:253], s[46:47] op_sel_hi:[1,0]\n"
    "v_pk_mul_f32 v[254:255], v[254:255], s[46:47] op_sel_hi:[1,0]\n"
    "v_cvt_pk_bf16_f32 v170, v252, v253\n"
    "v_cvt_pk_bf16_f32 v171, v254, v255\n"
    "ds_write_b128 v250, v[168:171] offset:10240\n"
    "ds_read_b128 a[8:11], v248 offset:0\n"
    "s_waitcnt vmcnt(12)\n"
    "s_mov_b32 s44, 16384\n"
    "s_add_u32 m0, s45, 65536\n"
    "s_nop 0\n"
    "buffer_load_dwordx4 v216, s[28:31], s44 offen offset:0 nt lds\n"
    "v_lshlrev_b32 v252, 16, v172\n"
    "v_and_b32 v253, 0xffff0000, v172\n"
    "v_lshlrev_b32 v254, 16, v173\n"
    "v_and_b32 v255, 0xffff0000, v173\n"
    "v_pk_mul_f32 v[252:253], v[252:253], s[46:47] op_sel_hi:[1,0]\n"
    "v_pk_mul_f32 v[254:255], v[254:255], s[46:47] op_sel_hi:[1,0]\n"
    "v_cvt_pk_bf16_f32 v172, v252, v253\n"
    "v_cvt_pk_bf16_f32 v173, v254, v255\n"
    "v_lshlrev_b32 v252, 16, v174\n"
    "v_and_b32 v253, 0xffff0000, v174\n"
    "v_lshlrev_b32 v254, 16, v175\n"
    "v_and_b32 v255, 0xffff0000, v175\n"
    "v_pk_mul_f32 v[252:253], v[252:253], s[46:47] op_sel_hi:[1,0]\n"
    "v_pk_mul_f32 v[254:255], v[254:255], s[46:47] op_sel_hi:[1,0]\n"
    "v_cvt_pk_bf16_f32 v174, v252, v253\n"
    "v_cvt_pk_bf16_f32 v175, v254, v255\n"
    "ds_write_b128 v250, v[172:175] offset:11264\n"
    "ds_read_b128 a[12:15], v249 offset:0\n"
    "s_waitcnt vmcnt(12)\n"
    "buffer_load_dwordx4 v217, s[28:31], s44 offen offset:1024 nt lds\n"
    "v_lshlrev_b32 v252, 16, v176\n"
    "v_and_b32 v253, 0xffff0000, v176\n"
    "v_lshlrev_b32 v254, 16, v177\n"
    "v_and_b32 v255, 0xffff0000, v177\n"
    "v_pk_mul_f32 v[252:253], v[252:253], s[46:47] op_sel_hi:[1,0]\n"
    "v_pk_mul_f32 v[254:255], v[254:255], s[46:47] op_sel_hi:[1,0]\n"
    "v_cvt_pk_bf16_f32 v176, v252, v253\n"
    "v_cvt_pk_bf16_f32 v177, v254, v255\n"
    "v_lshlrev_b32 v252, 16, v178\n"
    "v_and_b32 v253, 0xffff0000, v178\n"
    "v_lshlrev_b32 v254, 16, v179\n"
    "v_and_b32 v255, 0xffff0000, v179\n"
    "v_pk_mul_f32 v[252:253], v[252:253], s[46:47] op_sel_hi:[1,0]\n"
    "v_pk_mul_f32 v[254:255], v[254:255], s[46:47] op_sel_hi:[1,0]\n"
    "v_cvt_pk_bf16_f32 v178, v252, v253\n"
    "v_cvt_pk_bf16_f32 v179, v254, v255\n"
    "ds_write_b128 v250, v[176:179] offset:12288\n"
    "ds_read_b128 a[16:19], v246 offset:256\n"
    "s_waitcnt vmcnt(12)\n"
    "buffer_load_dwordx4 v218, s[28:31], s44 offen offset:2048 nt lds\n"
    "v_lshlrev_b32 v252, 16, v180\n"
    "v_and_b32 v253, 0xffff0000, v180\n"
    "v_lshlrev_b32 v254, 16, v181\n"
    "v_and_b32 v255, 0xffff0000, v181\n"
    "v_pk_mul_f32 v[252:253], v[252:253], s[46:47] op_sel_hi:[1,0]\n"
    "v_pk_mul_f32 v[254:255], v[254:255], s[46:47] op_sel_hi:[1,0]\n"
    "v_cvt_pk_bf16_f32 v180, v252, v253\n"
    "v_cvt_pk_bf16_f32 v181, v254, v255\n"
    "v_lshlrev_b32 v252, 16, v182\n"
    "v_and_b32 v253, 0xffff0000, v182\n"
    "v_lshlrev_b32 v254, 16, v183\n"
    "v_and_b32 v255, 0xffff0000, v183\n"
    "v_pk_mul_f32 v[252:253], v[252:253], s[46:47] op_sel_hi:[1,0]\n"
    "v_pk_mul_f32 v[254:255], v[254:255], s[46:47] op_sel_hi:[1,0]\n"
    "v_cvt_pk_bf16_f32 v182, v252, v253\n"
    "v_cvt_pk_bf16_f32 v183, v254, v255\n"
    "ds_write_b128 v250, v[180:183] offset:13312\n"
    "ds_read_b128 a[20:23], v247 offset:256\n"
    "s_waitcnt vmcnt(12)\n"
    "buffer_load_dwordx4 v219, s[28:31], s44 offen offset:3072 nt lds\n"
    "v_lshlrev_b32 v252, 16, v184\n"
    "v_and_b32 v253, 0xffff0000, v184\n"
    "v_lshlrev_b32 v254, 16, v185\n"
    "v_and_b32 v255, 0xffff0000, v185\n"
    "v_pk_mul_f32 v[252:253], v[252:253], s[46:47] op_sel_hi:[1,0]\n"
    "v_pk_mul_f32 v[254:255], v[254:255], s[46:47] op_sel_hi:[1,0]\n"
    "v_cvt_pk_bf16_f32 v184, v252, v253\n"
    "v_cvt_pk_bf16_f32 v185, v254, v255\n"
    "v_lshlrev_b32 v252, 16, v186\n"
    "v_and_b32 v253, 0xffff0000, v186\n"
    "v_lshlrev_b32 v254, 16, v187\n"
    "v_and_b32 v255, 0xffff0000, v187\n"
    "v_pk_mul_f32 v[252:253], v[252:253], s[46:47] op_sel_hi:[1,0]\n"
    "v_pk_mul_f32 v[254:255], v[254:255], s[46:47] op_sel_hi:[1,0]\n"
    "v_cvt_pk_bf16_f32 v186, v252, v253\n"
    "v_cvt_pk_bf16_f32 v187, v254, v255\n"
    "ds_write_b128 v250, v[184:187] offset:14336\n"
    "ds_read_b128 a[24:27], v248 offset:256\n"
    "s_waitcnt vmcnt(12)\n"
    "v_lshlrev_b32 v252, 16, v188\n"
    "v_and_b32 v253, 0xffff0000, v188\n"
    "v_lshlrev_b32 v254, 16, v189\n"
    "v_and_b32 v255, 0xffff0000, v189\n"
    "v_pk_mul_f32 v[252:253], v[252:253], s[46:47] op_sel_hi:[1,0]\n"
    "v_pk_mul_f32 v[254:255], v[254:255], s[46:47] op_sel_hi:[1,0]\n"
    "v_cvt_pk_bf16_f32 v188, v252, v253\n"
    "v_cvt_pk_bf16_f32 v189, v254, v255\n"
    "v_lshlrev_b32 v252, 16, v190\n"
    "v_and_b32 v253, 0xffff0000, v190\n"
    "v_lshlrev_b32 v254, 16, v191\n"
    "v_and_b32 v255, 0xffff0000, v191\n"
    "v_pk_mul_f32 v[252:253], v[252:253], s[46:47] op_sel_hi:[1,0]\n"
    "v_pk_mul_f32 v[254:255], v[254:255], s[46:47] op_sel_hi:[1,0]\n"
    "v_cvt_pk_bf16_f32 v190, v252, v253\n"
    "v_cvt_pk_bf16_f32 v191, v254, v255\n"
    "ds_write_b128 v250, v[188:191] offset:15360\n"
    "ds_read_b128 a[28:31], v249 offset:256\n"
    "ds_read_b128 a[32:35], v246 offset:8192\n"
    "ds_read_b128 a[36:39], v247 offset:8192\n"
    "ds_read_b128 a[40:43], v248 offset:8192\n"
    "ds_read_b128 a[44:47], v249 offset:8192\n"
    "ds_read_b128 a[48:51], v246 offset:8448\n"
    "ds_read_b128 a[52:55], v247 offset:8448\n"
    "ds_read_b128 a[56:59], v248 offset:8448\n"
    "ds_read_b128 a[60:63], v249 offset:8448\n"
    "s_mov_b32 s44, 32768\n"
    "s_add_u32 m0, s45, 32768\n"
    "s_waitcnt vmcnt(8)\n"
    "s_barrier\n"
    "ds_read_b128 a[64:67], v200 offset:0\n"
    "ds_read_b128 a[68:71], v201 offset:0\n"
    "ds_read_b128 a[72:75], v202 offset:0\n"
    "ds_read_b128 a[76:79], v203 offset:0\n"
    "ds_read_b128 a[80:83], v200 offset:256\n"
    "ds_read_b128 a[84:87], v201 offset:256\n"
    "ds_read_b128 a[88:91], v202 offset:256\n"
    "ds_read_b128 a[92:95], v203 offset:256\n"
    "s_waitcnt lgkmcnt(6)\n"
    "v_mfma_f32_16x16x32_bf16 v[128:131], a[64:67], a[0:3], 0\n"
    "ds_read_b128 a[96:99], v200 offset:8192\n"
    "v_mfma_f32_16x16x32_bf16 v[128:131], a[68:71], a[4:7], v[128:131]\n"
    "s_waitcnt lgkmcnt(5)\n"
    "ds_read_b128 a[100:103], v201 offset:8192\n"
    "v_mfma_f32_16x16x32_bf16 v[128:131], a[72:75], a[8:11], v[128:131]\n"
    "ds_read_b128 a[104:107], v202 offset:8192\n"
    "v_mfma_f32_16x16x32_bf16 v[128:131], a[76:79], a[12:15], v[128:131]\n"
    "s_waitcnt lgkmcnt(5)\n"
    "ds_read_b128 a[108:111], v203 offset:8192\n"
    "v_mfma_f32_16x16x32_bf16 v[128:131], a[80:83], a[16:19], v[128:131]\n"
    "ds_read_b128 a[112:115], v200 offset:8448\n"
    "v_mfma_f32_16x16x32_bf16 v[128:131], a[84:87], a[20:23], v[128:131]\n"
    "s_waitcnt lgkmcnt(5)\n"
    "ds_read_b128 a[116:119], v201 offset:8448\n"
    "v_mfma_f32_16x16x32_bf16 v[128:131], a[88:91], a[24:27], v[128:131]\n"
    "ds_read_b128 a[120:123], v202 offset:8448\n"
    "v_mfma_f32_16x16x32_bf16 v[128:131], a[92:95], a[28:31], v[128:131]\n"
    "ds_read_b128 a[124:127], v203 offset:8448\n"
    "buffer_load_dword v231, v226, s[40:43], s58 offen offset:8 sc1\n"
    "v_mfma_f32_16x16x32_bf16 v[136:139], a[92:95], a[60:63], 0\n"
    "buffer_load_dwordx4 v212, s[24:27], s44 offen offset:0 nt lds\n"
    "v_mfma_f32_16x16x32_bf16 v[136:139], a[88:91], a[56:59], v[136:139]\n"
    "v_mfma_f32_16x16x32_bf16 v[136:139], a[84:87], a[52:55], v[136:139]\n"
    "v_mfma_f32_16x16x32_bf16 v[136:139], a[80:83], a[48:51], v[136:139]\n"
    "buffer_load_dwordx4 v213, s[24:27], s44 offen offset:1024 nt lds\n"
    "v_mfma_f32_16x16x32_bf16 v[136:139], a[76:79], a[44:47], v[136:139]\n"
    "v_mfma_f32_16x16x32_bf16 v[136:139], a[72:75], a[40:43], v[136:139]\n"
    "v_mfma_f32_16x16x32_bf16 v[136:139], a[68:71], a[36:39], v[136:139]\n"
    "buffer_load_dwordx4 v214, s[24:27], s44 offen offset:2048 nt lds\n"
    "v_mfma_f32_16x16x32_bf16 v[136:139], a[64:67], a[32:35], v[136:139]\n"
    "s_waitcnt lgkmcnt(0)\n"
    "v_mfma_f32_16x16x32_bf16 v[140:143], a[96:99], a[32:35], 0\n"
    "v_mfma_f32_16x16x32_bf16 v[140:143], a[100:103], a[36:39], v[140:143]\n"
    "buffer_load_dwordx4 v215, s[24:27], s44 offen offset:3072 nt lds\n"
    "v_mfma_f32_16x16x32_bf16 v[140:143], a[104:107], a[40:43], v[140:143]\n"
    "v_mfma_f32_16x16x32_bf16 v[140:143], a[108:111], a[44:47], v[140:143]\n"
    "s_mov_b32 s44, 32768\n"
    "s_add_u32 m0, s45, 81920\n"
    "v_mfma_f32_16x16x32_bf16 v[140:143], a[112:115], a[48:51], v[140:143]\n"
    "buffer_load_dwordx4 v216, s[28:31], s44 offen offset:0 nt lds\n"
    "v_mfma_f32_16x16x32_bf16 v[140:143], a[116:119], a[52:55], v[140:143]\n"
    "v_mfma_f32_16x16x32_bf16 v[140:143], a[120:123], a[56:59], v[140:143]\n"
    "v_mfma_f32_16x16x32_bf16 v[140:143], a[124:127], a[60:63], v[140:143]\n"
    "buffer_load_dwordx4 v217, s[28:31], s44 offen offset:1024 nt lds\n"
    "v_mfma_f32_16x16x32_bf16 v[132:135], a[124:127], a[28:31], 0\n"
    "v_mfma_f32_16x16x32_bf16 v[132:135], a[120:123], a[24:27], v[132:135]\n"
    "v_mfma_f32_16x16x32_bf16 v[132:135], a[116:119], a[20:23], v[132:135]\n"
    "buffer_load_dwordx4 v218, s[28:31], s44 offen offset:2048 nt lds\n"
    "v_mfma_f32_16x16x32_bf16 v[132:135], a[112:115], a[16:19], v[132:135]\n"
    "v_mfma_f32_16x16x32_bf16 v[132:135], a[108:111], a[12:15], v[132:135]\n"
    "v_mfma_f32_16x16x32_bf16 v[132:135], a[104:107], a[8:11], v[132:135]\n"
    "buffer_load_dwordx4 v219, s[28:31], s44 offen offset:3072 nt lds\n"
    "v_mfma_f32_16x16x32_bf16 v[132:135], a[100:103], a[4:7], v[132:135]\n"
    "v_mfma_f32_16x16x32_bf16 v[132:135], a[96:99], a[0:3], v[132:135]\n"
    "s_mov_b32 s44, 49152\n"
    "s_add_u32 m0, s45, 98304\n"
    "s_waitcnt vmcnt(9)\n"
    "s_barrier\n"
    "ds_read_b128 a[64:67], v200 offset:16384\n"
    "ds_read_b128 a[68:71], v201 offset:16384\n"
    "ds_read_b128 a[72:75], v202 offset:16384\n"
    "ds_read_b128 a[76:79], v203 offset:16384\n"
    "ds_read_b128 a[80:83], v200 offset:16640\n"
    "ds_read_b128 a[84:87], v201 offset:16640\n"
    "ds_read_b128 a[88:91], v202 offset:16640\n"
    "ds_read_b128 a[92:95], v203 offset:16640\n"
    "v_max3_f32 v232, v128, v129, v130\n"
    "v_max3_f32 v233, v136, v137, v138\n"
    "v_max3_f32 v232, v232, v131, v132\n"
    "v_max3_f32 v233, v233, v139, v140\n"
    "v_max3_f32 v232, v232, v133, v134\n"
    "v_max3_f32 v233, v233, v141, v142\n"
    "v_max_f32 v232, v232, v135\n"
    "v_max_f32 v233, v233, v143\n"
    "s_nop 1\n"
    "v_permlane32_swap_b32 v232, v233\n"
    "v_max_f32 v232, v232, v233\n"
    "v_mov_b32 v234, v232\n"
    "s_nop 1\n"
    "v_permlane16_swap_b32 v232, v234\n"
    "v_max_f32 v232, v232, v234\n"
    "v_mov_b32 v233, v232\n"
    "s_nop 1\n"
    "v_permlane32_swap_b32 v232, v233\n"
    "v_max_f32 v232, 0xff61b1e6, v232\n"
    "v_max_f32 v233, 0xff61b1e6, v233\n"
    "v_sub_f32 v192, 0, v232\n"
    "v_sub_f32 v193, 0, v232\n"
    "v_sub_f32 v194, 0, v232\n"
    "v_sub_f32 v195, 0, v232\n"
    "v_sub_f32 v196, 0, v233\n"
    "v_sub_f32 v197, 0, v233\n"
    "v_sub_f32 v198, 0, v233\n"
    "v_sub_f32 v199, 0, v233\n"
    "s_waitcnt lgkmcnt(0)\n"
    "v_mfma_f32_16x16x32_bf16 v[144:147], a[64:67], a[0:3], v[192:195]\n"
    "ds_read_b128 a[96:99], v200 offset:24576\n"
    "v_add_f32 v128, v128, v192\n"
    "v_add_f32 v129, v129, v192\n"
    "v_mfma_f32_16x16x32_bf16 v[144:147], a[68:71], a[4:7], v[144:147]\n"
    "ds_read_b128 a[100:103], v201 offset:24576\n"
    "buffer_load_dwordx4 v216, s[28:31], s44 offen offset:0 nt lds\n"
    "v_exp_f32 v176, v128\n"
    "v_add_f32 v130, v130, v192\n"
    "v_mfma_f32_16x16x32_bf16 v[144:147], a[72:75], a[8:11], v[144:147]\n"
    "ds_read_b128 a[104:107], v202 offset:24576\n"
    "v_exp_f32 v177, v129\n"
    "v_add_f32 v131, v131, v192\n"
    "v_mfma_f32_16x16x32_bf16 v[144:147], a[76:79], a[12:15], v[144:147]\n"
    "ds_read_b128 a[108:111], v203 offset:24576\n"
    "v_exp_f32 v178, v130\n"
    "v_add_f32 v222, v176, v177\n"
    "v_mfma_f32_16x16x32_bf16 v[144:147], a[80:83], a[16:19], v[144:147]\n"
    "ds_read_b128 a[112:115], v200 offset:24832\n"
    "buffer_load_dwordx4 v217, s[28:31], s44 offen offset:1024 nt lds\n"
    "v_cvt_pk_bf16_f32 v160, v176, v177\n"
    "v_add_f32 v136, v136, v196\n"
    "v_mfma_f32_16x16x32_bf16 v[144:147], a[84:87], a[20:23], v[144:147]\n"
    "ds_read_b128 a[116:119], v201 offset:24832\n"
    "v_exp_f32 v179, v131\n"
    "v_add_f32 v222, v222, v178\n"
    "v_mfma_f32_16x16x32_bf16 v[144:147], a[88:91], a[24:27], v[144:147]\n"
    "ds_read_b128 a[120:123], v202 offset:24832\n"
    "v_add_f32 v137, v137, v196\n"
    "v_exp_f32 v180, v136\n"
    "v_mfma_f32_16x16x32_bf16 v[144:147], a[92:95], a[28:31], v[144:147]\n"
    "ds_read_b128 a[124:127], v203 offset:24832\n"
    "buffer_load_dwordx4 v218, s[28:31], s44 offen offset:2048 nt lds\n"
    "v_add_f32 v222, v222, v179\n"
    "v_cvt_pk_bf16_f32 v161, v178, v179\n"
    "v_mfma_f32_16x16x32_bf16 v[152:155], a[92:95], a[60:63], v[196:199]\n"
    "ds_read_b64_tr_b16 a[128:129], v204 offset:0\n"
    "ds_read_b64_tr_b16 a[130:131], v204 offset:8192\n"
    "v_add_f32 v138, v138, v196\n"
    "v_exp_f32 v181, v137\n"
    "v_mfma_f32_16x16x32_bf16 v[152:155], a[88:91], a[56:59], v[152:155]\n"
    "ds_read_b64_tr_b16 a[132:133], v205 offset:0\n"
    "v_add_f32 v139, v139, v196\n"
    "v_exp_f32 v182, v138\n"
    "v_mfma_f32_16x16x32_bf16 v[152:155], a[84:87], a[52:55], v[152:155]\n"
    "buffer_load_dwordx4 v219, s[28:31], s44 offen offset:3072 nt lds\n"
    "ds_read_b64_tr_b16 a[134:135], v205 offset:8192\n"
    "ds_read_b64_tr_b16 a[136:137], v206 offset:0\n"
    "v_add_f32 v223, v180, v181\n"
    "v_cvt_pk_bf16_f32 v164, v180, v181\n"
    "v_mfma_f32_16x16x32_bf16 v[152:155], a[80:83], a[48:51], v[152:155]\n"
    "ds_read_b64_tr_b16 a[138:139], v206 offset:8192\n"
    "v_add_f32 v132, v132, v192\n"
    "v_exp_f32 v183, v139\n"
    "v_mfma_f32_16x16x32_bf16 v[152:155], a[76:79], a[44:47], v[152:155]\n"
    "s_mov_b32 s44, 49152\n"
    "s_add_u32 m0, s45, 49152\n"
    "ds_read_b64_tr_b16 a[140:141], v207 offset:0\n"
    "v_add_f32 v223, v223, v182\n"
    "v_add_f32 v133, v133, v192\n"
    "v_mfma_f32_16x16x32_bf16 v[152:155], a[72:75], a[40:43], v[152:155]\n"
    "buffer_load_dwordx4 v212, s[24:27], s44 offen offset:0 nt lds\n"
    "ds_read_b64_tr_b16 a[142:143], v207 offset:8192\n"
    "ds_read_b64_tr_b16 a[144:145], v208 offset:0\n"
    "v_exp_f32 v176, v132\n"
    "v_add_f32 v223, v223, v183\n"
    "v_mfma_f32_16x16x32_bf16 v[152:155], a[68:71], a[36:39], v[152:155]\n"
    "ds_read_b64_tr_b16 a[146:147], v208 offset:8192\n"
    "v_cvt_pk_bf16_f32 v165, v182, v183\n"
    "v_add_f32 v134, v134, v192\n"
    "v_mfma_f32_16x16x32_bf16 v[152:155], a[64:67], a[32:35], v[152:155]\n"
    "s_waitcnt lgkmcnt(10)\n"
    "ds_read_b64_tr_b16 a[148:149], v209 offset:0\n"
    "ds_read_b64_tr_b16 a[150:151], v209 offset:8192\n"
    "v_exp_f32 v177, v133\n"
    "v_add_f32 v222, v222, v176\n"
    "v_mfma_f32_16x16x32_bf16 v[156:159], a[96:99], a[32:35], v[196:199]\n"
    "buffer_load_dwordx4 v213, s[24:27], s44 offen offset:1024 nt lds\n"
    "ds_read_b64_tr_b16 a[152:153], v210 offset:0\n"
    "v_add_f32 v135, v135, v192\n"
    "v_exp_f32 v178, v134\n"
    "v_mfma_f32_16x16x32_bf16 v[156:159], a[100:103], a[36:39], v[156:159]\n"
    "ds_read_b64_tr_b16 a[154:155], v210 offset:8192\n"
    "v_add_f32 v222, v222, v177\n"
    "v_cvt_pk_bf16_f32 v162, v176, v177\n"
    "v_mfma_f32_16x16x32_bf16 v[156:159], a[104:107], a[40:43], v[156:159]\n"
    "ds_read_b64_tr_b16 a[156:157], v211 offset:0\n"
    "ds_read_b64_tr_b16 a[158:159], v211 offset:8192\n"
    "v_add_f32 v140, v140, v196\n"
    "v_exp_f32 v179, v135\n"
    "v_mfma_f32_16x16x32_bf16 v[156:159], a[108:111], a[44:47], v[156:159]\n"
    "buffer_load_dwordx4 v214, s[24:27], s44 offen offset:2048 nt lds\n"
    "ds_read_b64_tr_b16 a[160:161], v204 offset:256\n"
    "v_add_f32 v222, v222, v178\n"
    "v_add_f32 v141, v141, v196\n"
    "v_mfma_f32_16x16x32_bf16 v[156:159], a[112:115], a[48:51], v[156:159]\n"
    "ds_read_b64_tr_b16 a[162:163], v204 offset:8448\n"
    "ds_read_b64_tr_b16 a[164:165], v205 offset:256\n"
    "v_exp_f32 v180, v140\n"
    "v_add_f32 v222, v222, v179\n"
    "v_mfma_f32_16x16x32_bf16 v[156:159], a[116:119], a[52:55], v[156:159]\n"
    "ds_read_b64_tr_b16 a[166:167], v205 offset:8448\n"
    "v_cvt_pk_bf16_f32 v163, v178, v179\n"
    "v_add_f32 v142, v142, v196\n"
    "v_mfma_f32_16x16x32_bf16 v[156:159], a[120:123], a[56:59], v[156:159]\n"
    "buffer_load_dwordx4 v215, s[24:27], s44 offen offset:3072 nt lds\n"
    "ds_read_b64_tr_b16 a[168:169], v206 offset:256\n"
    "v_exp_f32 v181, v141\n"
    "v_add_f32 v223, v223, v180\n"
    "v_mfma_f32_16x16x32_bf16 v[156:159], a[124:127], a[60:63], v[156:159]\n"
    "ds_read_b64_tr_b16 a[170:171], v206 offset:8448\n"
    "ds_read_b64_tr_b16 a[172:173], v207 offset:256\n"
    "v_add_f32 v143, v143, v196\n"
    "v_exp_f32 v182, v142\n"
    "v_mfma_f32_16x16x32_bf16 v[148:151], a[124:127], a[28:31], v[192:195]\n"
    "ds_read_b64_tr_b16 a[174:175], v207 offset:8448\n"
    "v_add_f32 v223, v223, v181\n"
    "v_cvt_pk_bf16_f32 v166, v180, v181\n"
    "v_mfma_f32_16x16x32_bf16 v[148:151], a[120:123], a[24:27], v[148:151]\n"
    "ds_read_b64_tr_b16 a[176:177], v208 offset:256\n"
    "ds_read_b64_tr_b16 a[178:179], v208 offset:8448\n"
    "v_exp_f32 v183, v143\n"
    "v_add_f32 v223, v223, v182\n"
    "v_mfma_f32_16x16x32_bf16 v[148:151], a[116:119], a[20:23], v[148:151]\n"
    "s_mov_b32 s44, 65536\n"
    "ds_read_b64_tr_b16 a[180:181], v209 offset:256\n"
    "v_add_f32 v223, v223, v183\n"
    "v_cvt_pk_bf16_f32 v167, v182, v183\n"
    "v_mfma_f32_16x16x32_bf16 v[148:151], a[112:115], a[16:19], v[148:151]\n"
    "s_add_u32 m0, s45, 114688\n"
    "ds_read_b64_tr_b16 a[182:183], v209 offset:8448\n"
    "s_add_u32 s100, s100, 8192\n"
    "buffer_load_dwordx4 v[244:247], v230, s[28:31], s100 offen\n"
    "v_mov_b32 v224, 0\n"
    "v_mfma_f32_16x16x32_bf16 v[148:151], a[108:111], a[12:15], v[148:151]\n"
    "ds_read_b64_tr_b16 a[184:185], v210 offset:256\n"
    "ds_read_b64_tr_b16 a[186:187], v210 offset:8448\n"
    "v_mfma_f32_16x16x32_bf16 v[148:151], a[104:107], a[8:11], v[148:151]\n"
    "ds_read_b64_tr_b16 a[188:189], v211 offset:256\n"
    "s_add_u32 s57, s57, 8192\n"
    "buffer_load_dwordx4 v[244:247], v227, s[24:27], s57 offen\n"
    "v_mfma_f32_16x16x32_bf16 v[148:151], a[100:103], a[4:7], v[148:151]\n"
    "s_branch .Lstub_b0_%=\n"
    ".Lret_b0_%=:\n"
    "ds_read_b64_tr_b16 a[190:191], v211 offset:8448\n"
    "v_mov_b32 v220, 0\n"
    "v_mov_b32 v221, 0\n"
    "v_mfma_f32_16x16x32_bf16 v[148:151], a[96:99], a[0:3], v[148:151]\n"
    "s_waitcnt vmcnt(10) lgkmcnt(15)\n"
    "v_bfe_u32 v231, v231, v229, 1\n"
    "v_cmp_eq_u32 vcc, 0, v231\n"
    "s_barrier\n"
    "s_pack_ll_b32_b16 s53, vcc_lo, vcc_hi\n"
    "v_mov_b32 v231, s53\n"
    "buffer_store_dword v231, v228, s[36:39], s58 offen offset:12 sc1\n"
    "s_branch .Lenter_%=\n"
    ".p2align 6\n"
    "s_nop 0\n"
    "s_nop 0\n"
    ".Lloop_%=:\n"
    "v_mfma_f32_16x16x32_bf16 v[0:3], v[168:171], a[128:131], v[0:3]\n"
    "ds_read_b128 a[64:67], v200 offset:49152\n"
    "v_add_f32 v220, v220, v222\n"
    "v_mfma_f32_16x16x32_bf16 v[4:7], v[172:175], a[128:131], v[4:7]\n"
    "v_exp_f32 v176, v128\n"
    "v_mfma_f32_16x16x32_bf16 v[12:15], v[172:175], a[132:135], v[12:15]\n"
    "buffer_load_dwordx4 v216, s[28:31], s44 offen offset:0 nt lds\n"
    "v_add_f32 v221, v221, v223\n"
    "v_mfma_f32_16x16x32_bf16 v[8:11], v[168:171], a[132:135], v[8:11]\n"
    "ds_read_b128 a[68:71], v201 offset:49152\n"
    "v_mfma_f32_16x16x32_bf16 v[16:19], v[168:171], a[136:139], v[16:19]\n"
    "v_exp_f32 v177, v129\n"
    "v_mfma_f32_16x16x32_bf16 v[20:23], v[172:175], a[136:139], v[20:23]\n"
    "ds_read_b128 a[72:75], v202 offset:49152\n"
    "v_add_f32 v222, v176, v177\n"
    "v_mfma_f32_16x16x32_bf16 v[28:31], v[172:175], a[140:143], v[28:31]\n"
    "ds_read_b128 a[76:79], v203 offset:49152\n"
    "v_cvt_pk_bf16_f32 v160, v176, v177\n"
    "v_mfma_f32_16x16x32_bf16 v[24:27], v[168:171], a[140:143], v[24:27]\n"
    "ds_read_b64_tr_b16 a[128:129], v204 offset:32768\n"
    "ds_read_b64_tr_b16 a[130:131], v204 offset:40960\n"
    "v_mfma_f32_16x16x32_bf16 v[32:35], v[168:171], a[144:147], v[32:35]\n"
    "v_exp_f32 v178, v130\n"
    "v_mfma_f32_16x16x32_bf16 v[36:39], v[172:175], a[144:147], v[36:39]\n"
    "ds_read_b128 a[80:83], v200 offset:49408\n"
    "v_add_f32 v222, v222, v178\n"
    "v_mfma_f32_16x16x32_bf16 v[44:47], v[172:175], a[148:151], v[44:47]\n"
    "buffer_load_dwordx4 v217, s[28:31], s44 offen offset:1024 nt lds\n"
    "ds_read_b64_tr_b16 a[132:133], v205 offset:32768\n"
    "v_mfma_f32_16x16x32_bf16 v[40:43], v[168:171], a[148:151], v[40:43]\n"
    "v_exp_f32 v179, v131\n"
    "v_mfma_f32_16x16x32_bf16 v[48:51], v[168:171], a[152:155], v[48:51]\n"
    "ds_read_b128 a[84:87], v201 offset:49408\n"
    "v_add_f32 v222, v222, v179\n"
    "v_mfma_f32_16x16x32_bf16 v[52:55], v[172:175], a[152:155], v[52:55]\n"
    "s_waitcnt lgkmcnt(9)\n"
    "ds_read_b128 a[88:91], v202 offset:49408\n"
    "v_cvt_pk_bf16_f32 v161, v178, v179\n"
    "v_mfma_f32_16x16x32_bf16 v[60:63], v[172:175], a[156:159], v[60:63]\n"
    "v_exp_f32 v180, v136\n"
    "v_mfma_f32_16x16x32_bf16 v[56:59], v[168:171], a[156:159], v[56:59]\n"
    "ds_read_b64_tr_b16 a[134:135], v205 offset:40960\n"
    "ds_read_b64_tr_b16 a[136:137], v206 offset:32768\n"
    "v_mfma_f32_16x16x32_bf16 v[64:67], v[168:171], a[160:163], v[64:67]\n"
    "ds_read_b128 a[92:95], v203 offset:49408\n"
    "v_mfma_f32_16x16x32_bf16 v[68:71], v[172:175], a[160:163], v[68:71]\n"
    "buffer_load_dwordx4 v218, s[28:31], s44 offen offset:2048 nt lds\n"
    "ds_read_b64_tr_b16 a[138:139], v206 offset:40960\n"
    "v_mfma_f32_16x16x32_bf16 v[76:79], v[172:175], a[164:167], v[76:79]\n"
    "v_exp_f32 v181, v137\n"
    "v_mfma_f32_16x16x32_bf16 v[72:75], v[168:171], a[164:167], v[72:75]\n"
    "ds_read_b128 a[96:99], v200 offset:57344\n"
    "v_add_f32 v223, v180, v181\n"
    "v_mfma_f32_16x16x32_bf16 v[80:83], v[168:171], a[168:171], v[80:83]\n"
    "ds_read_b128 a[100:103], v201 offset:57344\n"
    "v_cvt_pk_bf16_f32 v164, v180, v181\n"
    "v_mfma_f32_16x16x32_bf16 v[84:87], v[172:175], a[168:171], v[84:87]\n"
    "v_exp_f32 v182, v138\n"
    "v_mfma_f32_16x16x32_bf16 v[92:95], v[172:175], a[172:175], v[92:95]\n"
    "ds_read_b128 a[104:107], v202 offset:57344\n"
    "v_add_f32 v223, v223, v182\n"
    "v_mfma_f32_16x16x32_bf16 v[88:91], v[168:171], a[172:175], v[88:91]\n"
    "ds_read_b64_tr_b16 a[140:141], v207 offset:32768\n"
    "ds_read_b64_tr_b16 a[142:143], v207 offset:40960\n"
    "v_mfma_f32_16x16x32_bf16 v[96:99], v[168:171], a[176:179], v[96:99]\n"
    "v_exp_f32 v183, v139\n"
    "v_mfma_f32_16x16x32_bf16 v[100:103], v[172:175], a[176:179], v[100:103]\n"
    "ds_read_b128 a[108:111], v203 offset:57344\n"
    "v_add_f32 v223, v223, v183\n"
    "v_mfma_f32_16x16x32_bf16 v[108:111], v[172:175], a[180:183], v[108:111]\n"
    "buffer_load_dwordx4 v219, s[28:31], s44 offen offset:3072 nt lds\n"
    "v_cvt_pk_bf16_f32 v165, v182, v183\n"
    "v_mfma_f32_16x16x32_bf16 v[104:107], v[168:171], a[180:183], v[104:107]\n"
    "ds_read_b64_tr_b16 a[144:145], v208 offset:32768\n"
    "ds_read_b64_tr_b16 a[146:147], v208 offset:40960\n"
    "v_mfma_f32_16x16x32_bf16 v[112:115], v[168:171], a[184:187], v[112:115]\n"
    "v_exp_f32 v176, v132\n"
    "v_mfma_f32_16x16x32_bf16 v[116:119], v[172:175], a[184:187], v[116:119]\n"
    "ds_read_b128 a[112:115], v200 offset:57600\n"
    "v_add_f32 v222, v222, v176\n"
    "v_mfma_f32_16x16x32_bf16 v[124:127], v[172:175], a[188:191], v[124:127]\n"
    "ds_read_b64_tr_b16 a[148:149], v209 offset:32768\n"
    "ds_read_b64_tr_b16 a[150:151], v209 offset:40960\n"
    "v_mfma_f32_16x16x32_bf16 v[120:123], v[168:171], a[188:191], v[120:123]\n"
    "s_waitcnt lgkmcnt(12)\n"
    "v_exp_f32 v177, v133\n"
    "v_mfma_f32_16x16x32_bf16 v[144:147], a[64:67], a[0:3], v[192:195]\n"
    "s_add_u32 m0, s45, 16384\n"
    "ds_read_b128 a[116:119], v201 offset:57600\n"
    "v_add_f32 v222, v222, v177\n"
    "v_mfma_f32_16x16x32_bf16 v[144:147], a[68:71], a[4:7], v[144:147]\n"
    "buffer_load_dwordx4 v212, s[24:27], s44 offen offset:0 nt lds\n"
    "v_cvt_pk_bf16_f32 v162, v176, v177\n"
    "v_mfma_f32_16x16x32_bf16 v[144:147], a[72:75], a[8:11], v[144:147]\n"
    "v_exp_f32 v178, v134\n"
    "v_mfma_f32_16x16x32_bf16 v[144:147], a[76:79], a[12:15], v[144:147]\n"
    "ds_read_b128 a[120:123], v202 offset:57600\n"
    "v_add_f32 v222, v222, v178\n"
    "v_mfma_f32_16x16x32_bf16 v[144:147], a[80:83], a[16:19], v[144:147]\n"
    "ds_read_b128 a[124:127], v203 offset:57600\n"
    "v_mfma_f32_16x16x32_bf16 v[144:147], a[84:87], a[20:23], v[144:147]\n"
    "ds_read_b64_tr_b16 a[152:153], v210 offset:32768\n"
    "ds_read_b64_tr_b16 a[154:155], v210 offset:40960\n"
    "v_mfma_f32_16x16x32_bf16 v[144:147], a[88:91], a[24:27], v[144:147]\n"
    "v_exp_f32 v179, v135\n"
    "v_mfma_f32_16x16x32_bf16 v[144:147], a[92:95], a[28:31], v[144:147]\n"
    "ds_read_b64_tr_b16 a[156:157], v211 offset:32768\n"
    "v_add_f32 v222, v222, v179\n"
    "v_mfma_f32_16x16x32_bf16 v[152:155], a[92:95], a[60:63], v[196:199]\n"
    "ds_read_b64_tr_b16 a[158:159], v211 offset:40960\n"
    "v_cvt_pk_bf16_f32 v163, v178, v179\n"
    "v_mfma_f32_16x16x32_bf16 v[152:155], a[88:91], a[56:59], v[152:155]\n"
    "v_exp_f32 v180, v140\n"
    "v_mfma_f32_16x16x32_bf16 v[152:155], a[84:87], a[52:55], v[152:155]\n"
    "buffer_load_dwordx4 v213, s[24:27], s44 offen offset:1024 nt lds\n"
    "v_add_f32 v223, v223, v180\n"
    "v_mfma_f32_16x16x32_bf16 v[152:155], a[80:83], a[48:51], v[152:155]\n"
    "ds_read_b64_tr_b16 a[160:161], v204 offset:33024\n"
    "ds_read_b64_tr_b16 a[162:163], v204 offset:41216\n"
    "v_mfma_f32_16x16x32_bf16 v[152:155], a[76:79], a[44:47], v[152:155]\n"
    "v_exp_f32 v181, v141\n"
    "v_mfma_f32_16x16x32_bf16 v[152:155], a[72:75], a[40:43], v[152:155]\n"
    "ds_read_b64_tr_b16 a[164:165], v205 offset:33024\n"
    "v_add_f32 v223, v223, v181\n"
    "v_mfma_f32_16x16x32_bf16 v[152:155], a[68:71], a[36:39], v[152:155]\n"
    "ds_read_b64_tr_b16 a[166:167], v205 offset:41216\n"
    "v_cvt_pk_bf16_f32 v166, v180, v181\n"
    "v_mfma_f32_16x16x32_bf16 v[152:155], a[64:67], a[32:35], v[152:155]\n"
    "s_waitcnt lgkmcnt(8)\n"
    "ds_read_b64_tr_b16 a[168:169], v206 offset:33024\n"
    "ds_read_b64_tr_b16 a[170:171], v206 offset:41216\n"
    "v_mfma_f32_16x16x32_bf16 v[156:159], a[96:99], a[32:35], v[196:199]\n"
    "v_exp_f32 v182, v142\n"
    "v_mfma_f32_16x16x32_bf16 v[156:159], a[100:103], a[36:39], v[156:159]\n"
    "buffer_load_dwordx4 v214, s[24:27], s44 offen offset:2048 nt lds\n"
    "v_add_f32 v223, v223, v182\n"
    "v_mfma_f32_16x16x32_bf16 v[156:159], a[104:107], a[40:43], v[156:159]\n"
    "ds_read_b64_tr_b16 a[172:173], v207 offset:33024\n"
    "ds_read_b64_tr_b16 a[174:175], v207 offset:41216\n"
    "v_mfma_f32_16x16x32_bf16 v[156:159], a[108:111], a[44:47], v[156:159]\n"
    "v_exp_f32 v183, v143\n"
    "v_mfma_f32_16x16x32_bf16 v[156:159], a[112:115], a[48:51], v[156:159]\n"
    "ds_read_b64_tr_b16 a[176:177], v208 offset:33024\n"
    "v_add_f32 v223, v223, v183\n"
    "v_mfma_f32_16x16x32_bf16 v[156:159], a[116:119], a[52:55], v[156:159]\n"
    "ds_read_b64_tr_b16 a[178:179], v208 offset:41216\n"
    "v_add_f32 v224, v222, v223\n"
    "v_mfma_f32_16x16x32_bf16 v[156:159], a[120:123], a[56:59], v[156:159]\n"
    "ds_read_b64_tr_b16 a[180:181], v209 offset:33024\n"
    "v_cmp_nge_f32 vcc, s47, v224\n"
    "v_mfma_f32_16x16x32_bf16 v[156:159], a[124:127], a[60:63], v[156:159]\n"
    "ds_read_b64_tr_b16 a[182:183], v209 offset:41216\n"
    "v_cvt_pk_bf16_f32 v167, v182, v183\n"
    "v_mfma_f32_16x16x32_bf16 v[148:151], a[124:127], a[28:31], v[192:195]\n"
    "ds_read_b64_tr_b16 a[184:185], v210 offset:33024\n"
    "v_mfma_f32_16x16x32_bf16 v[148:151], a[120:123], a[24:27], v[148:151]\n"
    "buffer_load_dwordx4 v215, s[24:27], s44 offen offset:3072 nt lds\n"
    "v_mfma_f32_16x16x32_bf16 v[148:151], a[116:119], a[20:23], v[148:151]\n"
    "s_cbranch_vccnz .Lstub_p2_%=\n"
    ".Lret_p2_%=:\n"
    "v_mfma_f32_16x16x32_bf16 v[148:151], a[112:115], a[16:19], v[148:151]\n"
    "s_add_u32 s44, s44, 16384\n"
    "ds_read_b64_tr_b16 a[186:187], v210 offset:41216\n"
    "v_mfma_f32_16x16x32_bf16 v[148:151], a[108:111], a[12:15], v[148:151]\n"
    "s_add_u32 m0, s45, 81920\n"
    "ds_read_b64_tr_b16 a[188:189], v211 offset:33024\n"
    "v_mfma_f32_16x16x32_bf16 v[148:151], a[104:107], a[8:11], v[148:151]\n"
    "ds_read_b64_tr_b16 a[190:191], v211 offset:41216\n"
    "v_mfma_f32_16x16x32_bf16 v[148:151], a[100:103], a[4:7], v[148:151]\n"
    "v_mfma_f32_16x16x32_bf16 v[148:151], a[96:99], a[0:3], v[148:151]\n"
    "s_waitcnt vmcnt(9) lgkmcnt(15)\n"
    "s_barrier\n"
    "v_mfma_f32_16x16x32_bf16 v[0:3], v[160:163], a[128:131], v[0:3]\n"
    "ds_read_b128 a[64:67], v200 offset:0\n"
    "v_add_f32 v220, v220, v222\n"
    "v_mfma_f32_16x16x32_bf16 v[4:7], v[164:167], a[128:131], v[4:7]\n"
    "s_add_u32 s57, s57, 8192\n"
    "v_exp_f32 v176, v144\n"
    "v_mfma_f32_16x16x32_bf16 v[12:15], v[164:167], a[132:135], v[12:15]\n"
    "buffer_load_dwordx4 v216, s[28:31], s44 offen offset:0 nt lds\n"
    "v_add_f32 v221, v221, v223\n"
    "v_mfma_f32_16x16x32_bf16 v[8:11], v[160:163], a[132:135], v[8:11]\n"
    "ds_read_b128 a[68:71], v201 offset:0\n"
    "v_mfma_f32_16x16x32_bf16 v[16:19], v[160:163], a[136:139], v[16:19]\n"
    "v_exp_f32 v177, v145\n"
    "v_mfma_f32_16x16x32_bf16 v[20:23], v[164:167], a[136:139], v[20:23]\n"
    "ds_read_b128 a[72:75], v202 offset:0\n"
    "v_add_f32 v222, v176, v177\n"
    "v_mfma_f32_16x16x32_bf16 v[28:31], v[164:167], a[140:143], v[28:31]\n"
    "ds_read_b128 a[76:79], v203 offset:0\n"
    "v_cvt_pk_bf16_f32 v168, v176, v177\n"
    "v_mfma_f32_16x16x32_bf16 v[24:27], v[160:163], a[140:143], v[24:27]\n"
    "ds_read_b64_tr_b16 a[128:129], v204 offset:49152\n"
    "ds_read_b64_tr_b16 a[130:131], v204 offset:57344\n"
    "v_mfma_f32_16x16x32_bf16 v[32:35], v[160:163], a[144:147], v[32:35]\n"
    "v_exp_f32 v178, v146\n"
    "v_mfma_f32_16x16x32_bf16 v[36:39], v[164:167], a[144:147], v[36:39]\n"
    "ds_read_b128 a[80:83], v200 offset:256\n"
    "v_add_f32 v222, v222, v178\n"
    "v_mfma_f32_16x16x32_bf16 v[44:47], v[164:167], a[148:151], v[44:47]\n"
    "buffer_load_dwordx4 v217, s[28:31], s44 offen offset:1024 nt lds\n"
    "ds_read_b64_tr_b16 a[132:133], v205 offset:49152\n"
    "v_mfma_f32_16x16x32_bf16 v[40:43], v[160:163], a[148:151], v[40:43]\n"
    "v_exp_f32 v179, v147\n"
    "v_mfma_f32_16x16x32_bf16 v[48:51], v[160:163], a[152:155], v[48:51]\n"
    "ds_read_b128 a[84:87], v201 offset:256\n"
    "v_add_f32 v222, v222, v179\n"
    "v_mfma_f32_16x16x32_bf16 v[52:55], v[164:167], a[152:155], v[52:55]\n"
    "s_waitcnt lgkmcnt(9)\n"
    "ds_read_b128 a[88:91], v202 offset:256\n"
    "v_cvt_pk_bf16_f32 v169, v178, v179\n"
    "v_mfma_f32_16x16x32_bf16 v[60:63], v[164:167], a[156:159], v[60:63]\n"
    "v_exp_f32 v180, v152\n"
    "v_mfma_f32_16x16x32_bf16 v[56:59], v[160:163], a[156:159], v[56:59]\n"
    "ds_read_b64_tr_b16 a[134:135], v205 offset:57344\n"
    "ds_read_b64_tr_b16 a[136:137], v206 offset:49152\n"
    "v_mfma_f32_16x16x32_bf16 v[64:67], v[160:163], a[160:163], v[64:67]\n"
    "ds_read_b128 a[92:95], v203 offset:256\n"
    "v_mfma_f32_16x16x32_bf16 v[68:71], v[164:167], a[160:163], v[68:71]\n"
    "buffer_load_dwordx4 v218, s[28:31], s44 offen offset:2048 nt lds\n"
    "ds_read_b64_tr_b16 a[138:139], v206 offset:57344\n"
    "v_mfma_f32_16x16x32_bf16 v[76:79], v[164:167], a[164:167], v[76:79]\n"
    "v_exp_f32 v181, v153\n"
    "v_mfma_f32_16x16x32_bf16 v[72:75], v[160:163], a[164:167], v[72:75]\n"
    "ds_read_b128 a[96:99], v200 offset:8192\n"
    "v_add_f32 v223, v180, v181\n"
    "v_mfma_f32_16x16x32_bf16 v[80:83], v[160:163], a[168:171], v[80:83]\n"
    "ds_read_b128 a[100:103], v201 offset:8192\n"
    "v_cvt_pk_bf16_f32 v172, v180, v181\n"
    "v_mfma_f32_16x16x32_bf16 v[84:87], v[164:167], a[168:171], v[84:87]\n"
    "v_exp_f32 v182, v154\n"
    "v_mfma_f32_16x16x32_bf16 v[92:95], v[164:167], a[172:175], v[92:95]\n"
    "ds_read_b128 a[104:107], v202 offset:8192\n"
    "v_add_f32 v223, v223, v182\n"
    "v_mfma_f32_16x16x32_bf16 v[88:91], v[160:163], a[172:175], v[88:91]\n"
    "ds_read_b64_tr_b16 a[140:141], v207 offset:49152\n"
    "ds_read_b64_tr_b16 a[142:143], v207 offset:57344\n"
    "v_mfma_f32_16x16x32_bf16 v[96:99], v[160:163], a[176:179], v[96:99]\n"
    "v_exp_f32 v183, v155\n"
    "v_mfma_f32_16x16x32_bf16 v[100:103], v[164:167], a[176:179], v[100:103]\n"
    "ds_read_b128 a[108:111], v203 offset:8192\n"
    "v_add_f32 v223, v223, v183\n"
    "v_mfma_f32_16x16x32_bf16 v[108:111], v[164:167], a[180:183], v[108:111]\n"
    "buffer_load_dwordx4 v219, s[28:31], s44 offen offset:3072 nt lds\n"
    "v_cvt_pk_bf16_f32 v173, v182, v183\n"
    "v_mfma_f32_16x16x32_bf16 v[104:107], v[160:163], a[180:183], v[104:107]\n"
    "ds_read_b64_tr_b16 a[144:145], v208 offset:49152\n"
    "ds_read_b64_tr_b16 a[146:147], v208 offset:57344\n"
    "v_mfma_f32_16x16x32_bf16 v[112:115], v[160:163], a[184:187], v[112:115]\n"
    "v_exp_f32 v176, v148\n"
    "v_mfma_f32_16x16x32_bf16 v[116:119], v[164:167], a[184:187], v[116:119]\n"
    "ds_read_b128 a[112:115], v200 offset:8448\n"
    "v_add_f32 v222, v222, v176\n"
    "v_mfma_f32_16x16x32_bf16 v[124:127], v[164:167], a[188:191], v[124:127]\n"
    "ds_read_b64_tr_b16 a[148:149], v209 offset:49152\n"
    "ds_read_b64_tr_b16 a[150:151], v209 offset:57344\n"
    "v_mfma_f32_16x16x32_bf16 v[120:123], v[160:163], a[188:191], v[120:123]\n"
    "s_waitcnt lgkmcnt(12)\n"
    "v_exp_f32 v177, v149\n"
    "v_mfma_f32_16x16x32_bf16 v[128:131], a[64:67], a[0:3], v[192:195]\n"
    "s_add_u32 m0, s45, 32768\n"
    "ds_read_b128 a[116:119], v201 offset:8448\n"
    "v_add_f32 v222, v222, v177\n"
    "v_mfma_f32_16x16x32_bf16 v[128:131], a[68:71], a[4:7], v[128:131]\n"
    "buffer_load_dwordx4 v212, s[24:27], s44 offen offset:0 nt lds\n"
    "v_cvt_pk_bf16_f32 v170, v176, v177\n"
    "v_mfma_f32_16x16x32_bf16 v[128:131], a[72:75], a[8:11], v[128:131]\n"
    "v_exp_f32 v178, v150\n"
    "v_mfma_f32_16x16x32_bf16 v[128:131], a[76:79], a[12:15], v[128:131]\n"
    "ds_read_b128 a[120:123], v202 offset:8448\n"
    "v_add_f32 v222, v222, v178\n"
    "v_mfma_f32_16x16x32_bf16 v[128:131], a[80:83], a[16:19], v[128:131]\n"
    "ds_read_b128 a[124:127], v203 offset:8448\n"
    "v_mfma_f32_16x16x32_bf16 v[128:131], a[84:87], a[20:23], v[128:131]\n"
    "ds_read_b64_tr_b16 a[152:153], v210 offset:49152\n"
    "ds_read_b64_tr_b16 a[154:155], v210 offset:57344\n"
    "v_mfma_f32_16x16x32_bf16 v[128:131], a[88:91], a[24:27], v[128:131]\n"
    "v_exp_f32 v179, v151\n"
    "v_mfma_f32_16x16x32_bf16 v[128:131], a[92:95], a[28:31], v[128:131]\n"
    "ds_read_b64_tr_b16 a[156:157], v211 offset:49152\n"
    "v_add_f32 v222, v222, v179\n"
    "v_mfma_f32_16x16x32_bf16 v[136:139], a[92:95], a[60:63], v[196:199]\n"
    "ds_read_b64_tr_b16 a[158:159], v211 offset:57344\n"
    "v_cvt_pk_bf16_f32 v171, v178, v179\n"
    "v_mfma_f32_16x16x32_bf16 v[136:139], a[88:91], a[56:59], v[136:139]\n"
    "v_exp_f32 v180, v156\n"
    "v_mfma_f32_16x16x32_bf16 v[136:139], a[84:87], a[52:55], v[136:139]\n"
    "buffer_load_dwordx4 v213, s[24:27], s44 offen offset:1024 nt lds\n"
    "v_add_f32 v223, v223, v180\n"
    "v_mfma_f32_16x16x32_bf16 v[136:139], a[80:83], a[48:51], v[136:139]\n"
    "ds_read_b64_tr_b16 a[160:161], v204 offset:49408\n"
    "ds_read_b64_tr_b16 a[162:163], v204 offset:57600\n"
    "v_mfma_f32_16x16x32_bf16 v[136:139], a[76:79], a[44:47], v[136:139]\n"
    "v_exp_f32 v181, v157\n"
    "v_mfma_f32_16x16x32_bf16 v[136:139], a[72:75], a[40:43], v[136:139]\n"
    "ds_read_b64_tr_b16 a[164:165], v205 offset:49408\n"
    "v_add_f32 v223, v223, v181\n"
    "v_mfma_f32_16x16x32_bf16 v[136:139], a[68:71], a[36:39], v[136:139]\n"
    "ds_read_b64_tr_b16 a[166:167], v205 offset:57600\n"
    "v_cvt_pk_bf16_f32 v174, v180, v181\n"
    "v_mfma_f32_16x16x32_bf16 v[136:139], a[64:67], a[32:35], v[136:139]\n"
    "s_waitcnt lgkmcnt(8)\n"
    "ds_read_b64_tr_b16 a[168:169], v206 offset:49408\n"
    "ds_read_b64_tr_b16 a[170:171], v206 offset:57600\n"
    "v_mfma_f32_16x16x32_bf16 v[140:143], a[96:99], a[32:35], v[196:199]\n"
    "v_exp_f32 v182, v158\n"
    "v_mfma_f32_16x16x32_bf16 v[140:143], a[100:103], a[36:39], v[140:143]\n"
    "buffer_load_dwordx4 v214, s[24:27], s44 offen offset:2048 nt lds\n"
    "v_add_f32 v223, v223, v182\n"
    "v_mfma_f32_16x16x32_bf16 v[140:143], a[104:107], a[40:43], v[140:143]\n"
    "ds_read_b64_tr_b16 a[172:173], v207 offset:49408\n"
    "ds_read_b64_tr_b16 a[174:175], v207 offset:57600\n"
    "v_mfma_f32_16x16x32_bf16 v[140:143], a[108:111], a[44:47], v[140:143]\n"
    "v_exp_f32 v183, v159\n"
    "v_mfma_f32_16x16x32_bf16 v[140:143], a[112:115], a[48:51], v[140:143]\n"
    "ds_read_b64_tr_b16 a[176:177], v208 offset:49408\n"
    "v_add_f32 v223, v223, v183\n"
    "v_mfma_f32_16x16x32_bf16 v[140:143], a[116:119], a[52:55], v[140:143]\n"
    "ds_read_b64_tr_b16 a[178:179], v208 offset:57600\n"
    "v_add_f32 v224, v222, v223\n"
    "v_mfma_f32_16x16x32_bf16 v[140:143], a[120:123], a[56:59], v[140:143]\n"
    "ds_read_b64_tr_b16 a[180:181], v209 offset:49408\n"
    "v_cmp_nge_f32 vcc, s47, v224\n"
    "v_mfma_f32_16x16x32_bf16 v[140:143], a[124:127], a[60:63], v[140:143]\n"
    "ds_read_b64_tr_b16 a[182:183], v209 offset:57600\n"
    "v_cvt_pk_bf16_f32 v175, v182, v183\n"
    "v_mfma_f32_16x16x32_bf16 v[132:135], a[124:127], a[28:31], v[192:195]\n"
    "ds_read_b64_tr_b16 a[184:185], v210 offset:49408\n"
    "v_mfma_f32_16x16x32_bf16 v[132:135], a[120:123], a[24:27], v[132:135]\n"
    "buffer_load_dwordx4 v215, s[24:27], s44 offen offset:3072 nt lds\n"
    "v_mfma_f32_16x16x32_bf16 v[132:135], a[116:119], a[20:23], v[132:135]\n"
    "s_cbranch_vccnz .Lstub_p3_%=\n"
    ".Lret_p3_%=:\n"
    "v_mfma_f32_16x16x32_bf16 v[132:135], a[112:115], a[16:19], v[132:135]\n"
    "s_add_u32 s44, s44, 16384\n"
    "ds_read_b64_tr_b16 a[186:187], v210 offset:57600\n"
    "v_mfma_f32_16x16x32_bf16 v[132:135], a[108:111], a[12:15], v[132:135]\n"
    "s_add_u32 m0, s45, 98304\n"
    "ds_read_b64_tr_b16 a[188:189], v211 offset:49408\n"
    "v_mfma_f32_16x16x32_bf16 v[132:135], a[104:107], a[8:11], v[132:135]\n"
    "ds_read_b64_tr_b16 a[190:191], v211 offset:57600\n"
    "v_mfma_f32_16x16x32_bf16 v[132:135], a[100:103], a[4:7], v[132:135]\n"
    "buffer_load_dwordx4 v[244:247], v227, s[24:27], s57 offen\n"
    "v_mfma_f32_16x16x32_bf16 v[132:135], a[96:99], a[0:3], v[132:135]\n"
    "s_waitcnt vmcnt(9) lgkmcnt(15)\n"
    "s_barrier\n"
    "v_mfma_f32_16x16x32_bf16 v[0:3], v[168:171], a[128:131], v[0:3]\n"
    "ds_read_b128 a[64:67], v200 offset:16384\n"
    "v_add_f32 v220, v220, v222\n"
    "v_mfma_f32_16x16x32_bf16 v[4:7], v[172:175], a[128:131], v[4:7]\n"
    "v_exp_f32 v176, v128\n"
    "v_mfma_f32_16x16x32_bf16 v[12:15], v[172:175], a[132:135], v[12:15]\n"
    "buffer_load_dwordx4 v216, s[28:31], s44 offen offset:0 nt lds\n"
    "v_add_f32 v221, v221, v223\n"
    "v_mfma_f32_16x16x32_bf16 v[8:11], v[168:171], a[132:135], v[8:11]\n"
    "ds_read_b128 a[68:71], v201 offset:16384\n"
    "v_mfma_f32_16x16x32_bf16 v[16:19], v[168:171], a[136:139], v[16:19]\n"
    "v_exp_f32 v177, v129\n"
    "v_mfma_f32_16x16x32_bf16 v[20:23], v[172:175], a[136:139], v[20:23]\n"
    "ds_read_b128 a[72:75], v202 offset:16384\n"
    "v_add_f32 v222, v176, v177\n"
    "v_mfma_f32_16x16x32_bf16 v[28:31], v[172:175], a[140:143], v[28:31]\n"
    "ds_read_b128 a[76:79], v203 offset:16384\n"
    "v_cvt_pk_bf16_f32 v160, v176, v177\n"
    "v_mfma_f32_16x16x32_bf16 v[24:27], v[168:171], a[140:143], v[24:27]\n"
    "ds_read_b64_tr_b16 a[128:129], v204 offset:0\n"
    "ds_read_b64_tr_b16 a[130:131], v204 offset:8192\n"
    "v_mfma_f32_16x16x32_bf16 v[32:35], v[168:171], a[144:147], v[32:35]\n"
    "v_exp_f32 v178, v130\n"
    "v_mfma_f32_16x16x32_bf16 v[36:39], v[172:175], a[144:147], v[36:39]\n"
    "ds_read_b128 a[80:83], v200 offset:16640\n"
    "v_add_f32 v222, v222, v178\n"
    "v_mfma_f32_16x16x32_bf16 v[44:47], v[172:175], a[148:151], v[44:47]\n"
    "buffer_load_dwordx4 v217, s[28:31], s44 offen offset:1024 nt lds\n"
    "ds_read_b64_tr_b16 a[132:133], v205 offset:0\n"
    "v_mfma_f32_16x16x32_bf16 v[40:43], v[168:171], a[148:151], v[40:43]\n"
    "v_exp_f32 v179, v131\n"
    "v_mfma_f32_16x16x32_bf16 v[48:51], v[168:171], a[152:155], v[48:51]\n"
    "ds_read_b128 a[84:87], v201 offset:16640\n"
    "v_add_f32 v222, v222, v179\n"
    "v_mfma_f32_16x16x32_bf16 v[52:55], v[172:175], a[152:155], v[52:55]\n"
    "s_waitcnt lgkmcnt(9)\n"
    "ds_read_b128 a[88:91], v202 offset:16640\n"
    "v_cvt_pk_bf16_f32 v161, v178, v179\n"
    "v_mfma_f32_16x16x32_bf16 v[60:63], v[172:175], a[156:159], v[60:63]\n"
    "v_exp_f32 v180, v136\n"
    "v_mfma_f32_16x16x32_bf16 v[56:59], v[168:171], a[156:159], v[56:59]\n"
    "ds_read_b64_tr_b16 a[134:135], v205 offset:8192\n"
    "ds_read_b64_tr_b16 a[136:137], v206 offset:0\n"
    "v_mfma_f32_16x16x32_bf16 v[64:67], v[168:171], a[160:163], v[64:67]\n"
    "ds_read_b128 a[92:95], v203 offset:16640\n"
    "v_mfma_f32_16x16x32_bf16 v[68:71], v[172:175], a[160:163], v[68:71]\n"
    "buffer_load_dwordx4 v218, s[28:31], s44 offen offset:2048 nt lds\n"
    "ds_read_b64_tr_b16 a[138:139], v206 offset:8192\n"
    "v_mfma_f32_16x16x32_bf16 v[76:79], v[172:175], a[164:167], v[76:79]\n"
    "v_exp_f32 v181, v137\n"
    "v_mfma_f32_16x16x32_bf16 v[72:75], v[168:171], a[164:167], v[72:75]\n"
    "ds_read_b128 a[96:99], v200 offset:24576\n"
    "v_add_f32 v223, v180, v181\n"
    "v_mfma_f32_16x16x32_bf16 v[80:83], v[168:171], a[168:171], v[80:83]\n"
    "ds_read_b128 a[100:103], v201 offset:24576\n"
    "v_cvt_pk_bf16_f32 v164, v180, v181\n"
    "v_mfma_f32_16x16x32_bf16 v[84:87], v[172:175], a[168:171], v[84:87]\n"
    "v_exp_f32 v182, v138\n"
    "v_mfma_f32_16x16x32_bf16 v[92:95], v[172:175], a[172:175], v[92:95]\n"
    "ds_read_b128 a[104:107], v202 offset:24576\n"
    "v_add_f32 v223, v223, v182\n"
    "v_mfma_f32_16x16x32_bf16 v[88:91], v[168:171], a[172:175], v[88:91]\n"
    "ds_read_b64_tr_b16 a[140:141], v207 offset:0\n"
    "ds_read_b64_tr_b16 a[142:143], v207 offset:8192\n"
    "v_mfma_f32_16x16x32_bf16 v[96:99], v[168:171], a[176:179], v[96:99]\n"
    "v_exp_f32 v183, v139\n"
    "v_mfma_f32_16x16x32_bf16 v[100:103], v[172:175], a[176:179], v[100:103]\n"
    "ds_read_b128 a[108:111], v203 offset:24576\n"
    "v_add_f32 v223, v223, v183\n"
    "v_mfma_f32_16x16x32_bf16 v[108:111], v[172:175], a[180:183], v[108:111]\n"
    "buffer_load_dwordx4 v219, s[28:31], s44 offen offset:3072 nt lds\n"
    "v_cvt_pk_bf16_f32 v165, v182, v183\n"
    "v_mfma_f32_16x16x32_bf16 v[104:107], v[168:171], a[180:183], v[104:107]\n"
    "ds_read_b64_tr_b16 a[144:145], v208 offset:0\n"
    "ds_read_b64_tr_b16 a[146:147], v208 offset:8192\n"
    "v_mfma_f32_16x16x32_bf16 v[112:115], v[168:171], a[184:187], v[112:115]\n"
    "v_exp_f32 v176, v132\n"
    "v_mfma_f32_16x16x32_bf16 v[116:119], v[172:175], a[184:187], v[116:119]\n"
    "ds_read_b128 a[112:115], v200 offset:24832\n"
    "v_add_f32 v222, v222, v176\n"
    "v_mfma_f32_16x16x32_bf16 v[124:127], v[172:175], a[188:191], v[124:127]\n"
    "ds_read_b64_tr_b16 a[148:149], v209 offset:0\n"
    "ds_read_b64_tr_b16 a[150:151], v209 offset:8192\n"
    "v_mfma_f32_16x16x32_bf16 v[120:123], v[168:171], a[188:191], v[120:123]\n"
    "s_waitcnt lgkmcnt(12)\n"
    "v_exp_f32 v177, v133\n"
    "v_mfma_f32_16x16x32_bf16 v[144:147], a[64:67], a[0:3], v[192:195]\n"
    "s_add_u32 m0, s45, 49152\n"
    "ds_read_b128 a[116:119], v201 offset:24832\n"
    "v_add_f32 v222, v222, v177\n"
    "v_mfma_f32_16x16x32_bf16 v[144:147], a[68:71], a[4:7], v[144:147]\n"
    "buffer_load_dwordx4 v212, s[24:27], s44 offen offset:0 nt lds\n"
    "v_cvt_pk_bf16_f32 v162, v176, v177\n"
    "v_mfma_f32_16x16x32_bf16 v[144:147], a[72:75], a[8:11], v[144:147]\n"
    "v_exp_f32 v178, v134\n"
    "v_mfma_f32_16x16x32_bf16 v[144:147], a[76:79], a[12:15], v[144:147]\n"
    "ds_read_b128 a[120:123], v202 offset:24832\n"
    "v_add_f32 v222, v222, v178\n"
    "v_mfma_f32_16x16x32_bf16 v[144:147], a[80:83], a[16:19], v[144:147]\n"
    "ds_read_b128 a[124:127], v203 offset:24832\n"
    "v_mfma_f32_16x16x32_bf16 v[144:147], a[84:87], a[20:23], v[144:147]\n"
    "ds_read_b64_tr_b16 a[152:153], v210 offset:0\n"
    "ds_read_b64_tr_b16 a[154:155], v210 offset:8192\n"
    "v_mfma_f32_16x16x32_bf16 v[144:147], a[88:91], a[24:27], v[144:147]\n"
    "v_exp_f32 v179, v135\n"
    "v_mfma_f32_16x16x32_bf16 v[144:147], a[92:95], a[28:31], v[144:147]\n"
    "ds_read_b64_tr_b16 a[156:157], v211 offset:0\n"
    "v_add_f32 v222, v222, v179\n"
    "v_mfma_f32_16x16x32_bf16 v[152:155], a[92:95], a[60:63], v[196:199]\n"
    "ds_read_b64_tr_b16 a[158:159], v211 offset:8192\n"
    "v_cvt_pk_bf16_f32 v163, v178, v179\n"
    "v_mfma_f32_16x16x32_bf16 v[152:155], a[88:91], a[56:59], v[152:155]\n"
    "v_exp_f32 v180, v140\n"
    "v_mfma_f32_16x16x32_bf16 v[152:155], a[84:87], a[52:55], v[152:155]\n"
    "buffer_load_dwordx4 v213, s[24:27], s44 offen offset:1024 nt lds\n"
    "v_add_f32 v223, v223, v180\n"
    "v_mfma_f32_16x16x32_bf16 v[152:155], a[80:83], a[48:51], v[152:155]\n"
    "ds_read_b64_tr_b16 a[160:161], v204 offset:256\n"
    "ds_read_b64_tr_b16 a[162:163], v204 offset:8448\n"
    "v_mfma_f32_16x16x32_bf16 v[152:155], a[76:79], a[44:47], v[152:155]\n"
    "v_exp_f32 v181, v141\n"
    "v_mfma_f32_16x16x32_bf16 v[152:155], a[72:75], a[40:43], v[152:155]\n"
    "ds_read_b64_tr_b16 a[164:165], v205 offset:256\n"
    "v_add_f32 v223, v223, v181\n"
    "v_mfma_f32_16x16x32_bf16 v[152:155], a[68:71], a[36:39], v[152:155]\n"
    "ds_read_b64_tr_b16 a[166:167], v205 offset:8448\n"
    "v_cvt_pk_bf16_f32 v166, v180, v181\n"
    "v_mfma_f32_16x16x32_bf16 v[152:155], a[64:67], a[32:35], v[152:155]\n"
    "s_waitcnt lgkmcnt(8)\n"
    "ds_read_b64_tr_b16 a[168:169], v206 offset:256\n"
    "ds_read_b64_tr_b16 a[170:171], v206 offset:8448\n"
    "v_mfma_f32_16x16x32_bf16 v[156:159], a[96:99], a[32:35], v[196:199]\n"
    "v_exp_f32 v182, v142\n"
    "v_mfma_f32_16x16x32_bf16 v[156:159], a[100:103], a[36:39], v[156:159]\n"
    "buffer_load_dwordx4 v214, s[24:27], s44 offen offset:2048 nt lds\n"
    "v_add_f32 v223, v223, v182\n"
    "v_mfma_f32_16x16x32_bf16 v[156:159], a[104:107], a[40:43], v[156:159]\n"
    "ds_read_b64_tr_b16 a[172:173], v207 offset:256\n"
    "ds_read_b64_tr_b16 a[174:175], v207 offset:8448\n"
    "v_mfma_f32_16x16x32_bf16 v[156:159], a[108:111], a[44:47], v[156:159]\n"
    "v_exp_f32 v183, v143\n"
    "v_mfma_f32_16x16x32_bf16 v[156:159], a[112:115], a[48:51], v[156:159]\n"
    "ds_read_b64_tr_b16 a[176:177], v208 offset:256\n"
    "v_add_f32 v223, v223, v183\n"
    "v_mfma_f32_16x16x32_bf16 v[156:159], a[116:119], a[52:55], v[156:159]\n"
    "ds_read_b64_tr_b16 a[178:179], v208 offset:8448\n"
    "v_add_f32 v224, v222, v223\n"
    "v_mfma_f32_16x16x32_bf16 v[156:159], a[120:123], a[56:59], v[156:159]\n"
    "ds_read_b64_tr_b16 a[180:181], v209 offset:256\n"
    "v_cmp_nge_f32 vcc, s47, v224\n"
    "v_mfma_f32_16x16x32_bf16 v[156:159], a[124:127], a[60:63], v[156:159]\n"
    "ds_read_b64_tr_b16 a[182:183], v209 offset:8448\n"
    "v_cvt_pk_bf16_f32 v167, v182, v183\n"
    "v_mfma_f32_16x16x32_bf16 v[148:151], a[124:127], a[28:31], v[192:195]\n"
    "ds_read_b64_tr_b16 a[184:185], v210 offset:256\n"
    "v_mfma_f32_16x16x32_bf16 v[148:151], a[120:123], a[24:27], v[148:151]\n"
    "buffer_load_dwordx4 v215, s[24:27], s44 offen offset:3072 nt lds\n"
    "v_mfma_f32_16x16x32_bf16 v[148:151], a[116:119], a[20:23], v[148:151]\n"
    "s_cbranch_vccnz .Lstub_p0_%=\n"
    ".Lret_p0_%=:\n"
    "v_mfma_f32_16x16x32_bf16 v[148:151], a[112:115], a[16:19], v[148:151]\n"
    "s_add_u32 s44, s44, 16384\n"
    "ds_read_b64_tr_b16 a[186:187], v210 offset:8448\n"
    "v_mfma_f32_16x16x32_bf16 v[148:151], a[108:111], a[12:15], v[148:151]\n"
    "s_add_u32 m0, s45, 114688\n"
    "ds_read_b64_tr_b16 a[188:189], v211 offset:256\n"
    "v_mfma_f32_16x16x32_bf16 v[148:151], a[104:107], a[8:11], v[148:151]\n"
    "ds_read_b64_tr_b16 a[190:191], v211 offset:8448\n"
    "v_mfma_f32_16x16x32_bf16 v[148:151], a[100:103], a[4:7], v[148:151]\n"
    "v_mfma_f32_16x16x32_bf16 v[148:151], a[96:99], a[0:3], v[148:151]\n"
    "s_waitcnt vmcnt(9) lgkmcnt(15)\n"
    "s_barrier\n"
    ".Lenter_%=:\n"
    "v_mfma_f32_16x16x32_bf16 v[0:3], v[160:163], a[128:131], v[0:3]\n"
    "ds_read_b128 a[64:67], v200 offset:32768\n"
    "v_add_f32 v220, v220, v222\n"
    "v_mfma_f32_16x16x32_bf16 v[4:7], v[164:167], a[128:131], v[4:7]\n"
    "s_add_u32 s100, s100, 8192\n"
    "v_exp_f32 v176, v144\n"
    "v_mfma_f32_16x16x32_bf16 v[12:15], v[164:167], a[132:135], v[12:15]\n"
    "buffer_load_dwordx4 v216, s[28:31], s44 offen offset:0 nt lds\n"
    "v_add_f32 v221, v221, v223\n"
    "v_mfma_f32_16x16x32_bf16 v[8:11], v[160:163], a[132:135], v[8:11]\n"
    "ds_read_b128 a[68:71], v201 offset:32768\n"
    "v_mfma_f32_16x16x32_bf16 v[16:19], v[160:163], a[136:139], v[16:19]\n"
    "v_exp_f32 v177, v145\n"
    "v_mfma_f32_16x16x32_bf16 v[20:23], v[164:167], a[136:139], v[20:23]\n"
    "ds_read_b128 a[72:75], v202 offset:32768\n"
    "v_add_f32 v222, v176, v177\n"
    "v_mfma_f32_16x16x32_bf16 v[28:31], v[164:167], a[140:143], v[28:31]\n"
    "ds_read_b128 a[76:79], v203 offset:32768\n"
    "v_cvt_pk_bf16_f32 v168, v176, v177\n"
    "v_mfma_f32_16x16x32_bf16 v[24:27], v[160:163], a[140:143], v[24:27]\n"
    "ds_read_b64_tr_b16 a[128:129], v204 offset:16384\n"
    "ds_read_b64_tr_b16 a[130:131], v204 offset:24576\n"
    "v_mfma_f32_16x16x32_bf16 v[32:35], v[160:163], a[144:147], v[32:35]\n"
    "v_exp_f32 v178, v146\n"
    "v_mfma_f32_16x16x32_bf16 v[36:39], v[164:167], a[144:147], v[36:39]\n"
    "ds_read_b128 a[80:83], v200 offset:33024\n"
    "v_add_f32 v222, v222, v178\n"
    "v_mfma_f32_16x16x32_bf16 v[44:47], v[164:167], a[148:151], v[44:47]\n"
    "buffer_load_dwordx4 v217, s[28:31], s44 offen offset:1024 nt lds\n"
    "ds_read_b64_tr_b16 a[132:133], v205 offset:16384\n"
    "v_mfma_f32_16x16x32_bf16 v[40:43], v[160:163], a[148:151], v[40:43]\n"
    "v_exp_f32 v179, v147\n"
    "v_mfma_f32_16x16x32_bf16 v[48:51], v[160:163], a[152:155], v[48:51]\n"
    "ds_read_b128 a[84:87], v201 offset:33024\n"
    "v_add_f32 v222, v222, v179\n"
    "v_mfma_f32_16x16x32_bf16 v[52:55], v[164:167], a[152:155], v[52:55]\n"
    "s_waitcnt lgkmcnt(9)\n"
    "ds_read_b128 a[88:91], v202 offset:33024\n"
    "v_cvt_pk_bf16_f32 v169, v178, v179\n"
    "v_mfma_f32_16x16x32_bf16 v[60:63], v[164:167], a[156:159], v[60:63]\n"
    "v_exp_f32 v180, v152\n"
    "v_mfma_f32_16x16x32_bf16 v[56:59], v[160:163], a[156:159], v[56:59]\n"
    "ds_read_b64_tr_b16 a[134:135], v205 offset:24576\n"
    "ds_read_b64_tr_b16 a[136:137], v206 offset:16384\n"
    "v_mfma_f32_16x16x32_bf16 v[64:67], v[160:163], a[160:163], v[64:67]\n"
    "ds_read_b128 a[92:95], v203 offset:33024\n"
    "v_mfma_f32_16x16x32_bf16 v[68:71], v[164:167], a[160:163], v[68:71]\n"
    "buffer_load_dwordx4 v218, s[28:31], s44 offen offset:2048 nt lds\n"
    "ds_read_b64_tr_b16 a[138:139], v206 offset:24576\n"
    "v_mfma_f32_16x16x32_bf16 v[76:79], v[164:167], a[164:167], v[76:79]\n"
    "v_exp_f32 v181, v153\n"
    "v_mfma_f32_16x16x32_bf16 v[72:75], v[160:163], a[164:167], v[72:75]\n"
    "ds_read_b128 a[96:99], v200 offset:40960\n"
    "v_add_f32 v223, v180, v181\n"
    "v_mfma_f32_16x16x32_bf16 v[80:83], v[160:163], a[168:171], v[80:83]\n"
    "ds_read_b128 a[100:103], v201 offset:40960\n"
    "v_cvt_pk_bf16_f32 v172, v180, v181\n"
    "v_mfma_f32_16x16x32_bf16 v[84:87], v[164:167], a[168:171], v[84:87]\n"
    "v_exp_f32 v182, v154\n"
    "v_mfma_f32_16x16x32_bf16 v[92:95], v[164:167], a[172:175], v[92:95]\n"
    "ds_read_b128 a[104:107], v202 offset:40960\n"
    "v_add_f32 v223, v223, v182\n"
    "v_mfma_f32_16x16x32_bf16 v[88:91], v[160:163], a[172:175], v[88:91]\n"
    "ds_read_b64_tr_b16 a[140:141], v207 offset:16384\n"
    "ds_read_b64_tr_b16 a[142:143], v207 offset:24576\n"
    "v_mfma_f32_16x16x32_bf16 v[96:99], v[160:163], a[176:179], v[96:99]\n"
    "v_exp_f32 v183, v155\n"
    "v_mfma_f32_16x16x32_bf16 v[100:103], v[164:167], a[176:179], v[100:103]\n"
    "ds_read_b128 a[108:111], v203 offset:40960\n"
    "v_add_f32 v223, v223, v183\n"
    "v_mfma_f32_16x16x32_bf16 v[108:111], v[164:167], a[180:183], v[108:111]\n"
    "buffer_load_dwordx4 v219, s[28:31], s44 offen offset:3072 nt lds\n"
    "v_cvt_pk_bf16_f32 v173, v182, v183\n"
    "v_mfma_f32_16x16x32_bf16 v[104:107], v[160:163], a[180:183], v[104:107]\n"
    "ds_read_b64_tr_b16 a[144:145], v208 offset:16384\n"
    "ds_read_b64_tr_b16 a[146:147], v208 offset:24576\n"
    "v_mfma_f32_16x16x32_bf16 v[112:115], v[160:163], a[184:187], v[112:115]\n"
    "v_exp_f32 v176, v148\n"
    "v_mfma_f32_16x16x32_bf16 v[116:119], v[164:167], a[184:187], v[116:119]\n"
    "ds_read_b128 a[112:115], v200 offset:41216\n"
    "v_add_f32 v222, v222, v176\n"
    "v_mfma_f32_16x16x32_bf16 v[124:127], v[164:167], a[188:191], v[124:127]\n"
    "ds_read_b64_tr_b16 a[148:149], v209 offset:16384\n"
    "ds_read_b64_tr_b16 a[150:151], v209 offset:24576\n"
    "v_mfma_f32_16x16x32_bf16 v[120:123], v[160:163], a[188:191], v[120:123]\n"
    "s_waitcnt lgkmcnt(12)\n"
    "v_exp_f32 v177, v149\n"
    "v_mfma_f32_16x16x32_bf16 v[128:131], a[64:67], a[0:3], v[192:195]\n"
    "s_add_u32 m0, s45, 0\n"
    "ds_read_b128 a[116:119], v201 offset:41216\n"
    "v_add_f32 v222, v222, v177\n"
    "v_mfma_f32_16x16x32_bf16 v[128:131], a[68:71], a[4:7], v[128:131]\n"
    "buffer_load_dwordx4 v212, s[24:27], s44 offen offset:0 nt lds\n"
    "v_cvt_pk_bf16_f32 v170, v176, v177\n"
    "v_mfma_f32_16x16x32_bf16 v[128:131], a[72:75], a[8:11], v[128:131]\n"
    "v_exp_f32 v178, v150\n"
    "v_mfma_f32_16x16x32_bf16 v[128:131], a[76:79], a[12:15], v[128:131]\n"
    "ds_read_b128 a[120:123], v202 offset:41216\n"
    "v_add_f32 v222, v222, v178\n"
    "v_mfma_f32_16x16x32_bf16 v[128:131], a[80:83], a[16:19], v[128:131]\n"
    "ds_read_b128 a[124:127], v203 offset:41216\n"
    "v_mfma_f32_16x16x32_bf16 v[128:131], a[84:87], a[20:23], v[128:131]\n"
    "ds_read_b64_tr_b16 a[152:153], v210 offset:16384\n"
    "ds_read_b64_tr_b16 a[154:155], v210 offset:24576\n"
    "v_mfma_f32_16x16x32_bf16 v[128:131], a[88:91], a[24:27], v[128:131]\n"
    "v_exp_f32 v179, v151\n"
    "v_mfma_f32_16x16x32_bf16 v[128:131], a[92:95], a[28:31], v[128:131]\n"
    "ds_read_b64_tr_b16 a[156:157], v211 offset:16384\n"
    "v_add_f32 v222, v222, v179\n"
    "v_mfma_f32_16x16x32_bf16 v[136:139], a[92:95], a[60:63], v[196:199]\n"
    "ds_read_b64_tr_b16 a[158:159], v211 offset:24576\n"
    "v_cvt_pk_bf16_f32 v171, v178, v179\n"
    "v_mfma_f32_16x16x32_bf16 v[136:139], a[88:91], a[56:59], v[136:139]\n"
    "v_exp_f32 v180, v156\n"
    "v_mfma_f32_16x16x32_bf16 v[136:139], a[84:87], a[52:55], v[136:139]\n"
    "buffer_load_dwordx4 v213, s[24:27], s44 offen offset:1024 nt lds\n"
    "v_add_f32 v223, v223, v180\n"
    "v_mfma_f32_16x16x32_bf16 v[136:139], a[80:83], a[48:51], v[136:139]\n"
    "ds_read_b64_tr_b16 a[160:161], v204 offset:16640\n"
    "ds_read_b64_tr_b16 a[162:163], v204 offset:24832\n"
    "v_mfma_f32_16x16x32_bf16 v[136:139], a[76:79], a[44:47], v[136:139]\n"
    "v_exp_f32 v181, v157\n"
    "v_mfma_f32_16x16x32_bf16 v[136:139], a[72:75], a[40:43], v[136:139]\n"
    "ds_read_b64_tr_b16 a[164:165], v205 offset:16640\n"
    "v_add_f32 v223, v223, v181\n"
    "v_mfma_f32_16x16x32_bf16 v[136:139], a[68:71], a[36:39], v[136:139]\n"
    "ds_read_b64_tr_b16 a[166:167], v205 offset:24832\n"
    "v_cvt_pk_bf16_f32 v174, v180, v181\n"
    "v_mfma_f32_16x16x32_bf16 v[136:139], a[64:67], a[32:35], v[136:139]\n"
    "s_waitcnt lgkmcnt(8)\n"
    "ds_read_b64_tr_b16 a[168:169], v206 offset:16640\n"
    "ds_read_b64_tr_b16 a[170:171], v206 offset:24832\n"
    "v_mfma_f32_16x16x32_bf16 v[140:143], a[96:99], a[32:35], v[196:199]\n"
    "v_exp_f32 v182, v158\n"
    "v_mfma_f32_16x16x32_bf16 v[140:143], a[100:103], a[36:39], v[140:143]\n"
    "buffer_load_dwordx4 v214, s[24:27], s44 offen offset:2048 nt lds\n"
    "v_add_f32 v223, v223, v182\n"
    "v_mfma_f32_16x16x32_bf16 v[140:143], a[104:107], a[40:43], v[140:143]\n"
    "ds_read_b64_tr_b16 a[172:173], v207 offset:16640\n"
    "ds_read_b64_tr_b16 a[174:175], v207 offset:24832\n"
    "v_mfma_f32_16x16x32_bf16 v[140:143], a[108:111], a[44:47], v[140:143]\n"
    "v_exp_f32 v183, v159\n"
    "v_mfma_f32_16x16x32_bf16 v[140:143], a[112:115], a[48:51], v[140:143]\n"
    "ds_read_b64_tr_b16 a[176:177], v208 offset:16640\n"
    "v_add_f32 v223, v223, v183\n"
    "v_mfma_f32_16x16x32_bf16 v[140:143], a[116:119], a[52:55], v[140:143]\n"
    "ds_read_b64_tr_b16 a[178:179], v208 offset:24832\n"
    "v_add_f32 v224, v222, v223\n"
    "v_mfma_f32_16x16x32_bf16 v[140:143], a[120:123], a[56:59], v[140:143]\n"
    "ds_read_b64_tr_b16 a[180:181], v209 offset:16640\n"
    "v_cmp_nge_f32 vcc, s47, v224\n"
    "v_mfma_f32_16x16x32_bf16 v[140:143], a[124:127], a[60:63], v[140:143]\n"
    "ds_read_b64_tr_b16 a[182:183], v209 offset:24832\n"
    "v_cvt_pk_bf16_f32 v175, v182, v183\n"
    "v_mfma_f32_16x16x32_bf16 v[132:135], a[124:127], a[28:31], v[192:195]\n"
    "ds_read_b64_tr_b16 a[184:185], v210 offset:16640\n"
    "v_mfma_f32_16x16x32_bf16 v[132:135], a[120:123], a[24:27], v[132:135]\n"
    "buffer_load_dwordx4 v215, s[24:27], s44 offen offset:3072 nt lds\n"
    "v_mfma_f32_16x16x32_bf16 v[132:135], a[116:119], a[20:23], v[132:135]\n"
    "s_cbranch_vccnz .Lstub_p1_%=\n"
    ".Lret_p1_%=:\n"
    "v_mfma_f32_16x16x32_bf16 v[132:135], a[112:115], a[16:19], v[132:135]\n"
    "s_add_u32 s44, s44, 16384\n"
    "ds_read_b64_tr_b16 a[186:187], v210 offset:24832\n"
    "v_mfma_f32_16x16x32_bf16 v[132:135], a[108:111], a[12:15], v[132:135]\n"
    "s_add_u32 m0, s45, 65536\n"
    "ds_read_b64_tr_b16 a[188:189], v211 offset:16640\n"
    "v_mfma_f32_16x16x32_bf16 v[132:135], a[104:107], a[8:11], v[132:135]\n"
    "s_sub_u32 s48, s48, 1\n"
    "ds_read_b64_tr_b16 a[190:191], v211 offset:24832\n"
    "v_mfma_f32_16x16x32_bf16 v[132:135], a[100:103], a[4:7], v[132:135]\n"
    "buffer_load_dwordx4 v[244:247], v230, s[28:31], s100 offen\n"
    "v_mfma_f32_16x16x32_bf16 v[132:135], a[96:99], a[0:3], v[132:135]\n"
    "s_waitcnt vmcnt(9) lgkmcnt(15)\n"
    "s_barrier\n"
    "s_cbranch_scc0 .Lloop_%=\n"
    "v_mfma_f32_16x16x32_bf16 v[0:3], v[168:171], a[128:131], v[0:3]\n"
    "ds_read_b128 a[64:67], v200 offset:49152\n"
    "v_add_f32 v220, v220, v222\n"
    "v_mfma_f32_16x16x32_bf16 v[4:7], v[172:175], a[128:131], v[4:7]\n"
    "ds_read_b128 a[68:71], v201 offset:49152\n"
    "v_add_f32 v221, v221, v223\n"
    "v_mfma_f32_16x16x32_bf16 v[12:15], v[172:175], a[132:135], v[12:15]\n"
    "v_exp_f32 v176, v128\n"
    "v_mfma_f32_16x16x32_bf16 v[8:11], v[168:171], a[132:135], v[8:11]\n"
    "buffer_load_dwordx4 v216, s[28:31], s44 offen offset:0 nt lds\n"
    "ds_read_b128 a[72:75], v202 offset:49152\n"
    "v_mfma_f32_16x16x32_bf16 v[16:19], v[168:171], a[136:139], v[16:19]\n"
    "v_exp_f32 v177, v129\n"
    "v_mfma_f32_16x16x32_bf16 v[20:23], v[172:175], a[136:139], v[20:23]\n"
    "s_waitcnt lgkmcnt(15)\n"
    "ds_read_b128 a[76:79], v203 offset:49152\n"
    "v_add_f32 v222, v176, v177\n"
    "v_mfma_f32_16x16x32_bf16 v[28:31], v[172:175], a[140:143], v[28:31]\n"
    "ds_read_b128 a[80:83], v200 offset:49408\n"
    "v_cvt_pk_bf16_f32 v160, v176, v177\n"
    "v_mfma_f32_16x16x32_bf16 v[24:27], v[168:171], a[140:143], v[24:27]\n"
    "v_exp_f32 v178, v130\n"
    "v_mfma_f32_16x16x32_bf16 v[32:35], v[168:171], a[144:147], v[32:35]\n"
    "ds_read_b128 a[84:87], v201 offset:49408\n"
    "v_add_f32 v222, v222, v178\n"
    "v_mfma_f32_16x16x32_bf16 v[36:39], v[172:175], a[144:147], v[36:39]\n"
    "buffer_load_dwordx4 v217, s[28:31], s44 offen offset:1024 nt lds\n"
    "ds_read_b128 a[88:91], v202 offset:49408\n"
    "v_mfma_f32_16x16x32_bf16 v[44:47], v[172:175], a[148:151], v[44:47]\n"
    "v_exp_f32 v179, v131\n"
    "v_mfma_f32_16x16x32_bf16 v[40:43], v[168:171], a[148:151], v[40:43]\n"
    "ds_read_b128 a[92:95], v203 offset:49408\n"
    "v_add_f32 v222, v222, v179\n"
    "v_cvt_pk_bf16_f32 v161, v178, v179\n"
    "v_mfma_f32_16x16x32_bf16 v[48:51], v[168:171], a[152:155], v[48:51]\n"
    "v_exp_f32 v180, v136\n"
    "v_mfma_f32_16x16x32_bf16 v[52:55], v[172:175], a[152:155], v[52:55]\n"
    "buffer_load_dwordx4 v218, s[28:31], s44 offen offset:2048 nt lds\n"
    "ds_read_b128 a[96:99], v200 offset:57344\n"
    "v_mfma_f32_16x16x32_bf16 v[60:63], v[172:175], a[156:159], v[60:63]\n"
    "v_exp_f32 v181, v137\n"
    "v_mfma_f32_16x16x32_bf16 v[56:59], v[168:171], a[156:159], v[56:59]\n"
    "s_waitcnt lgkmcnt(5)\n"
    "ds_read_b128 a[100:103], v201 offset:57344\n"
    "v_add_f32 v223, v180, v181\n"
    "v_mfma_f32_16x16x32_bf16 v[144:147], a[64:67], a[0:3], v[192:195]\n"
    "ds_read_b128 a[104:107], v202 offset:57344\n"
    "v_cvt_pk_bf16_f32 v164, v180, v181\n"
    "v_mfma_f32_16x16x32_bf16 v[144:147], a[68:71], a[4:7], v[144:147]\n"
    "v_exp_f32 v182, v138\n"
    "v_mfma_f32_16x16x32_bf16 v[144:147], a[72:75], a[8:11], v[144:147]\n"
    "buffer_load_dwordx4 v219, s[28:31], s44 offen offset:3072 nt lds\n"
    "v_add_f32 v223, v223, v182\n"
    "v_mfma_f32_16x16x32_bf16 v[144:147], a[76:79], a[12:15], v[144:147]\n"
    "s_waitcnt lgkmcnt(3)\n"
    "v_exp_f32 v183, v139\n"
    "v_mfma_f32_16x16x32_bf16 v[144:147], a[80:83], a[16:19], v[144:147]\n"
    "ds_read_b128 a[108:111], v203 offset:57344\n"
    "v_add_f32 v223, v223, v183\n"
    "v_mfma_f32_16x16x32_bf16 v[144:147], a[84:87], a[20:23], v[144:147]\n"
    "ds_read_b128 a[112:115], v200 offset:57600\n"
    "v_cvt_pk_bf16_f32 v165, v182, v183\n"
    "v_mfma_f32_16x16x32_bf16 v[144:147], a[88:91], a[24:27], v[144:147]\n"
    "v_exp_f32 v176, v132\n"
    "v_mfma_f32_16x16x32_bf16 v[144:147], a[92:95], a[28:31], v[144:147]\n"
    "ds_read_b128 a[116:119], v201 offset:57600\n"
    "v_add_f32 v222, v222, v176\n"
    "v_mfma_f32_16x16x32_bf16 v[152:155], a[92:95], a[60:63], v[196:199]\n"
    "s_add_u32 m0, s45, 16384\n"
    "v_exp_f32 v177, v133\n"
    "v_mfma_f32_16x16x32_bf16 v[152:155], a[88:91], a[56:59], v[152:155]\n"
    "buffer_load_dwordx4 v212, s[24:27], s44 offen offset:0 nt lds\n"
    "ds_read_b128 a[120:123], v202 offset:57600\n"
    "v_add_f32 v222, v222, v177\n"
    "v_mfma_f32_16x16x32_bf16 v[152:155], a[84:87], a[52:55], v[152:155]\n"
    "ds_read_b128 a[124:127], v203 offset:57600\n"
    "v_cvt_pk_bf16_f32 v162, v176, v177\n"
    "v_mfma_f32_16x16x32_bf16 v[152:155], a[80:83], a[48:51], v[152:155]\n"
    "v_exp_f32 v178, v134\n"
    "v_mfma_f32_16x16x32_bf16 v[152:155], a[76:79], a[44:47], v[152:155]\n"
    "ds_read_b64_tr_b16 a[128:129], v204 offset:32768\n"
    "v_add_f32 v222, v222, v178\n"
    "v_mfma_f32_16x16x32_bf16 v[152:155], a[72:75], a[40:43], v[152:155]\n"
    "v_exp_f32 v179, v135\n"
    "v_mfma_f32_16x16x32_bf16 v[152:155], a[68:71], a[36:39], v[152:155]\n"
    "buffer_load_dwordx4 v213, s[24:27], s44 offen offset:1024 nt lds\n"
    "v_add_f32 v222, v222, v179\n"
    "v_cvt_pk_bf16_f32 v163, v178, v179\n"
    "v_mfma_f32_16x16x32_bf16 v[152:155], a[64:67], a[32:35], v[152:155]\n"
    "s_waitcnt lgkmcnt(1)\n"
    "v_exp_f32 v180, v140\n"
    "v_mfma_f32_16x16x32_bf16 v[156:159], a[96:99], a[32:35], v[196:199]\n"
    "ds_read_b64_tr_b16 a[130:131], v204 offset:40960\n"
    "ds_read_b64_tr_b16 a[132:133], v205 offset:32768\n"
    "v_add_f32 v223, v223, v180\n"
    "v_mfma_f32_16x16x32_bf16 v[156:159], a[100:103], a[36:39], v[156:159]\n"
    "v_exp_f32 v181, v141\n"
    "v_mfma_f32_16x16x32_bf16 v[156:159], a[104:107], a[40:43], v[156:159]\n"
    "ds_read_b64_tr_b16 a[134:135], v205 offset:40960\n"
    "v_add_f32 v223, v223, v181\n"
    "v_cvt_pk_bf16_f32 v166, v180, v181\n"
    "v_mfma_f32_16x16x32_bf16 v[156:159], a[108:111], a[44:47], v[156:159]\n"
    "v_exp_f32 v182, v142\n"
    "v_mfma_f32_16x16x32_bf16 v[156:159], a[112:115], a[48:51], v[156:159]\n"
    "buffer_load_dwordx4 v214, s[24:27], s44 offen offset:2048 nt lds\n"
    "v_add_f32 v223, v223, v182\n"
    "v_mfma_f32_16x16x32_bf16 v[156:159], a[116:119], a[52:55], v[156:159]\n"
    "v_exp_f32 v183, v143\n"
    "v_mfma_f32_16x16x32_bf16 v[156:159], a[120:123], a[56:59], v[156:159]\n"
    "v_add_f32 v223, v223, v183\n"
    "v_add_f32 v224, v222, v223\n"
    "v_mfma_f32_16x16x32_bf16 v[156:159], a[124:127], a[60:63], v[156:159]\n"
    "v_cmp_nge_f32 vcc, s47, v224\n"
    "v_cvt_pk_bf16_f32 v167, v182, v183\n"
    "v_mfma_f32_16x16x32_bf16 v[148:151], a[124:127], a[28:31], v[192:195]\n"
    "ds_read_b64_tr_b16 a[136:137], v206 offset:32768\n"
    "ds_read_b64_tr_b16 a[138:139], v206 offset:40960\n"
    "v_mfma_f32_16x16x32_bf16 v[148:151], a[120:123], a[24:27], v[148:151]\n"
    "buffer_load_dwordx4 v215, s[24:27], s44 offen offset:3072 nt lds\n"
    "ds_read_b64_tr_b16 a[140:141], v207 offset:32768\n"
    "ds_read_b64_tr_b16 a[142:143], v207 offset:40960\n"
    "v_mfma_f32_16x16x32_bf16 v[148:151], a[116:119], a[20:23], v[148:151]\n"
    "s_cbranch_vccnz .Lstub_q26_%=\n"
    ".Lret_q26_%=:\n"
    "v_mfma_f32_16x16x32_bf16 v[148:151], a[112:115], a[16:19], v[148:151]\n"
    "s_add_u32 s44, s44, 16384\n"
    "ds_read_b64_tr_b16 a[144:145], v208 offset:32768\n"
    "ds_read_b64_tr_b16 a[146:147], v208 offset:40960\n"
    "v_mfma_f32_16x16x32_bf16 v[148:151], a[108:111], a[12:15], v[148:151]\n"
    "s_add_u32 m0, s45, 81920\n"
    "ds_read_b64_tr_b16 a[148:149], v209 offset:32768\n"
    "ds_read_b64_tr_b16 a[150:151], v209 offset:40960\n"
    "v_mfma_f32_16x16x32_bf16 v[148:151], a[104:107], a[8:11], v[148:151]\n"
    "ds_read_b64_tr_b16 a[152:153], v210 offset:32768\n"
    "ds_read_b64_tr_b16 a[154:155], v210 offset:40960\n"
    "v_mfma_f32_16x16x32_bf16 v[148:151], a[100:103], a[4:7], v[148:151]\n"
    "ds_read_b64_tr_b16 a[156:157], v211 offset:32768\n"
    "ds_read_b64_tr_b16 a[158:159], v211 offset:40960\n"
    "v_mfma_f32_16x16x32_bf16 v[148:151], a[96:99], a[0:3], v[148:151]\n"
    "s_waitcnt vmcnt(9) lgkmcnt(10)\n"
    "s_barrier\n"
    "v_mfma_f32_16x16x32_bf16 v[0:3], v[160:163], a[128:131], v[0:3]\n"
    "ds_read_b128 a[64:67], v200 offset:0\n"
    "v_add_f32 v220, v220, v222\n"
    "v_mfma_f32_16x16x32_bf16 v[4:7], v[164:167], a[128:131], v[4:7]\n"
    "ds_read_b128 a[68:71], v201 offset:0\n"
    "v_add_f32 v221, v221, v223\n"
    "v_mfma_f32_16x16x32_bf16 v[12:15], v[164:167], a[132:135], v[12:15]\n"
    "v_exp_f32 v176, v144\n"
    "v_mfma_f32_16x16x32_bf16 v[8:11], v[160:163], a[132:135], v[8:11]\n"
    "buffer_load_dwordx4 v216, s[28:31], s44 offen offset:0 nt lds\n"
    "ds_read_b128 a[72:75], v202 offset:0\n"
    "v_mfma_f32_16x16x32_bf16 v[16:19], v[160:163], a[136:139], v[16:19]\n"
    "v_exp_f32 v177, v145\n"
    "v_mfma_f32_16x16x32_bf16 v[20:23], v[164:167], a[136:139], v[20:23]\n"
    "s_waitcnt lgkmcnt(3)\n"
    "ds_read_b128 a[76:79], v203 offset:0\n"
    "v_add_f32 v222, v176, v177\n"
    "v_mfma_f32_16x16x32_bf16 v[28:31], v[164:167], a[140:143], v[28:31]\n"
    "ds_read_b128 a[80:83], v200 offset:256\n"
    "v_cvt_pk_bf16_f32 v184, v176, v177\n"
    "v_mfma_f32_16x16x32_bf16 v[24:27], v[160:163], a[140:143], v[24:27]\n"
    "v_exp_f32 v178, v146\n"
    "v_mfma_f32_16x16x32_bf16 v[32:35], v[160:163], a[144:147], v[32:35]\n"
    "ds_read_b128 a[84:87], v201 offset:256\n"
    "v_add_f32 v222, v222, v178\n"
    "v_mfma_f32_16x16x32_bf16 v[36:39], v[164:167], a[144:147], v[36:39]\n"
    "buffer_load_dwordx4 v217, s[28:31], s44 offen offset:1024 nt lds\n"
    "ds_read_b128 a[88:91], v202 offset:256\n"
    "v_mfma_f32_16x16x32_bf16 v[44:47], v[164:167], a[148:151], v[44:47]\n"
    "v_exp_f32 v179, v147\n"
    "v_mfma_f32_16x16x32_bf16 v[40:43], v[160:163], a[148:151], v[40:43]\n"
    "ds_read_b128 a[92:95], v203 offset:256\n"
    "v_add_f32 v222, v222, v179\n"
    "v_cvt_pk_bf16_f32 v185, v178, v179\n"
    "v_mfma_f32_16x16x32_bf16 v[48:51], v[160:163], a[152:155], v[48:51]\n"
    "v_exp_f32 v180, v152\n"
    "v_mfma_f32_16x16x32_bf16 v[52:55], v[164:167], a[152:155], v[52:55]\n"
    "buffer_load_dwordx4 v218, s[28:31], s44 offen offset:2048 nt lds\n"
    "ds_read_b128 a[96:99], v200 offset:8192\n"
    "v_mfma_f32_16x16x32_bf16 v[60:63], v[164:167], a[156:159], v[60:63]\n"
    "v_exp_f32 v181, v153\n"
    "v_mfma_f32_16x16x32_bf16 v[56:59], v[160:163], a[156:159], v[56:59]\n"
    "s_waitcnt lgkmcnt(5)\n"
    "ds_read_b128 a[100:103], v201 offset:8192\n"
    "v_add_f32 v223, v180, v181\n"
    "v_mfma_f32_16x16x32_bf16 v[128:131], a[64:67], a[0:3], v[192:195]\n"
    "ds_read_b128 a[104:107], v202 offset:8192\n"
    "v_cvt_pk_bf16_f32 v188, v180, v181\n"
    "v_mfma_f32_16x16x32_bf16 v[128:131], a[68:71], a[4:7], v[128:131]\n"
    "v_exp_f32 v182, v154\n"
    "v_mfma_f32_16x16x32_bf16 v[128:131], a[72:75], a[8:11], v[128:131]\n"
    "buffer_load_dwordx4 v219, s[28:31], s44 offen offset:3072 nt lds\n"
    "v_add_f32 v223, v223, v182\n"
    "v_mfma_f32_16x16x32_bf16 v[128:131], a[76:79], a[12:15], v[128:131]\n"
    "s_waitcnt lgkmcnt(3)\n"
    "v_exp_f32 v183, v155\n"
    "v_mfma_f32_16x16x32_bf16 v[128:131], a[80:83], a[16:19], v[128:131]\n"
    "ds_read_b128 a[108:111], v203 offset:8192\n"
    "v_add_f32 v223, v223, v183\n"
    "v_mfma_f32_16x16x32_bf16 v[128:131], a[84:87], a[20:23], v[128:131]\n"
    "ds_read_b128 a[112:115], v200 offset:8448\n"
    "v_cvt_pk_bf16_f32 v189, v182, v183\n"
    "v_mfma_f32_16x16x32_bf16 v[128:131], a[88:91], a[24:27], v[128:131]\n"
    "v_exp_f32 v176, v148\n"
    "v_mfma_f32_16x16x32_bf16 v[128:131], a[92:95], a[28:31], v[128:131]\n"
    "ds_read_b128 a[116:119], v201 offset:8448\n"
    "v_add_f32 v222, v222, v176\n"
    "v_mfma_f32_16x16x32_bf16 v[136:139], a[92:95], a[60:63], v[196:199]\n"
    "s_add_u32 m0, s45, 32768\n"
    "v_exp_f32 v177, v149\n"
    "v_mfma_f32_16x16x32_bf16 v[136:139], a[88:91], a[56:59], v[136:139]\n"
    "buffer_load_dwordx4 v212, s[24:27], s44 offen offset:0 nt lds\n"
    "ds_read_b128 a[120:123], v202 offset:8448\n"
    "v_add_f32 v222, v222, v177\n"
    "v_mfma_f32_16x16x32_bf16 v[136:139], a[84:87], a[52:55], v[136:139]\n"
    "ds_read_b128 a[124:127], v203 offset:8448\n"
    "v_cvt_pk_bf16_f32 v186, v176, v177\n"
    "v_mfma_f32_16x16x32_bf16 v[136:139], a[80:83], a[48:51], v[136:139]\n"
    "v_exp_f32 v178, v150\n"
    "v_mfma_f32_16x16x32_bf16 v[136:139], a[76:79], a[44:47], v[136:139]\n"
    "ds_read_b64_tr_b16 a[128:129], v204 offset:49152\n"
    "v_add_f32 v222, v222, v178\n"
    "v_mfma_f32_16x16x32_bf16 v[136:139], a[72:75], a[40:43], v[136:139]\n"
    "v_exp_f32 v179, v151\n"
    "v_mfma_f32_16x16x32_bf16 v[136:139], a[68:71], a[36:39], v[136:139]\n"
    "buffer_load_dwordx4 v213, s[24:27], s44 offen offset:1024 nt lds\n"
    "v_add_f32 v222, v222, v179\n"
    "v_cvt_pk_bf16_f32 v187, v178, v179\n"
    "v_mfma_f32_16x16x32_bf16 v[136:139], a[64:67], a[32:35], v[136:139]\n"
    "s_waitcnt lgkmcnt(1)\n"
    "v_exp_f32 v180, v156\n"
    "v_mfma_f32_16x16x32_bf16 v[140:143], a[96:99], a[32:35], v[196:199]\n"
    "ds_read_b64_tr_b16 a[130:131], v204 offset:57344\n"
    "ds_read_b64_tr_b16 a[132:133], v205 offset:49152\n"
    "v_add_f32 v223, v223, v180\n"
    "v_mfma_f32_16x16x32_bf16 v[140:143], a[100:103], a[36:39], v[140:143]\n"
    "v_exp_f32 v181, v157\n"
    "v_mfma_f32_16x16x32_bf16 v[140:143], a[104:107], a[40:43], v[140:143]\n"
    "ds_read_b64_tr_b16 a[134:135], v205 offset:57344\n"
    "v_add_f32 v223, v223, v181\n"
    "v_cvt_pk_bf16_f32 v190, v180, v181\n"
    "v_mfma_f32_16x16x32_bf16 v[140:143], a[108:111], a[44:47], v[140:143]\n"
    "v_exp_f32 v182, v158\n"
    "v_mfma_f32_16x16x32_bf16 v[140:143], a[112:115], a[48:51], v[140:143]\n"
    "buffer_load_dwordx4 v214, s[24:27], s44 offen offset:2048 nt lds\n"
    "v_add_f32 v223, v223, v182\n"
    "v_mfma_f32_16x16x32_bf16 v[140:143], a[116:119], a[52:55], v[140:143]\n"
    "v_exp_f32 v183, v159\n"
    "v_mfma_f32_16x16x32_bf16 v[140:143], a[120:123], a[56:59], v[140:143]\n"
    "v_add_f32 v223, v223, v183\n"
    "v_add_f32 v224, v222, v223\n"
    "v_mfma_f32_16x16x32_bf16 v[140:143], a[124:127], a[60:63], v[140:143]\n"
    "v_cmp_nge_f32 vcc, s47, v224\n"
    "v_cvt_pk_bf16_f32 v191, v182, v183\n"
    "v_mfma_f32_16x16x32_bf16 v[132:135], a[124:127], a[28:31], v[192:195]\n"
    "ds_read_b64_tr_b16 a[136:137], v206 offset:49152\n"
    "ds_read_b64_tr_b16 a[138:139], v206 offset:57344\n"
    "v_mfma_f32_16x16x32_bf16 v[132:135], a[120:123], a[24:27], v[132:135]\n"
    "buffer_load_dwordx4 v215, s[24:27], s44 offen offset:3072 nt lds\n"
    "ds_read_b64_tr_b16 a[140:141], v207 offset:49152\n"
    "ds_read_b64_tr_b16 a[142:143], v207 offset:57344\n"
    "v_mfma_f32_16x16x32_bf16 v[132:135], a[116:119], a[20:23], v[132:135]\n"
    "s_cbranch_vccnz .Lstub_q27_%=\n"
    ".Lret_q27_%=:\n"
    "v_mfma_f32_16x16x32_bf16 v[132:135], a[112:115], a[16:19], v[132:135]\n"
    "s_add_u32 s44, s44, 16384\n"
    "ds_read_b64_tr_b16 a[144:145], v208 offset:49152\n"
    "ds_read_b64_tr_b16 a[146:147], v208 offset:57344\n"
    "v_mfma_f32_16x16x32_bf16 v[132:135], a[108:111], a[12:15], v[132:135]\n"
    "s_add_u32 m0, s45, 131072\n"
    "ds_read_b64_tr_b16 a[148:149], v209 offset:49152\n"
    "ds_read_b64_tr_b16 a[150:151], v209 offset:57344\n"
    "v_mfma_f32_16x16x32_bf16 v[132:135], a[104:107], a[8:11], v[132:135]\n"
    "ds_read_b64_tr_b16 a[152:153], v210 offset:49152\n"
    "ds_read_b64_tr_b16 a[154:155], v210 offset:57344\n"
    "v_mfma_f32_16x16x32_bf16 v[132:135], a[100:103], a[4:7], v[132:135]\n"
    "ds_read_b64_tr_b16 a[156:157], v211 offset:49152\n"
    "ds_read_b64_tr_b16 a[158:159], v211 offset:57344\n"
    "v_mfma_f32_16x16x32_bf16 v[132:135], a[96:99], a[0:3], v[132:135]\n"
    "s_waitcnt vmcnt(8) lgkmcnt(10)\n"
    "s_barrier\n"
    "v_mfma_f32_16x16x32_bf16 v[0:3], v[184:187], a[128:131], v[0:3]\n"
    "ds_read_b128 a[64:67], v200 offset:16384\n"
    "v_add_f32 v220, v220, v222\n"
    "v_mfma_f32_16x16x32_bf16 v[4:7], v[188:191], a[128:131], v[4:7]\n"
    "ds_read_b128 a[68:71], v201 offset:16384\n"
    "v_add_f32 v221, v221, v223\n"
    "v_mfma_f32_16x16x32_bf16 v[12:15], v[188:191], a[132:135], v[12:15]\n"
    "v_exp_f32 v176, v128\n"
    "v_mfma_f32_16x16x32_bf16 v[8:11], v[184:187], a[132:135], v[8:11]\n"
    "buffer_load_dwordx4 v216, s[28:31], s44 offen offset:0 nt lds\n"
    "ds_read_b128 a[72:75], v202 offset:16384\n"
    "v_mfma_f32_16x16x32_bf16 v[16:19], v[184:187], a[136:139], v[16:19]\n"
    "v_exp_f32 v177, v129\n"
    "v_mfma_f32_16x16x32_bf16 v[20:23], v[188:191], a[136:139], v[20:23]\n"
    "s_waitcnt lgkmcnt(3)\n"
    "ds_read_b128 a[76:79], v203 offset:16384\n"
    "v_add_f32 v222, v176, v177\n"
    "v_mfma_f32_16x16x32_bf16 v[28:31], v[188:191], a[140:143], v[28:31]\n"
    "ds_read_b128 a[80:83], v200 offset:16640\n"
    "v_cvt_pk_bf16_f32 v232, v176, v177\n"
    "v_mfma_f32_16x16x32_bf16 v[24:27], v[184:187], a[140:143], v[24:27]\n"
    "v_exp_f32 v178, v130\n"
    "v_mfma_f32_16x16x32_bf16 v[32:35], v[184:187], a[144:147], v[32:35]\n"
    "ds_read_b128 a[84:87], v201 offset:16640\n"
    "v_add_f32 v222, v222, v178\n"
    "v_mfma_f32_16x16x32_bf16 v[36:39], v[188:191], a[144:147], v[36:39]\n"
    "buffer_load_dwordx4 v217, s[28:31], s44 offen offset:1024 nt lds\n"
    "ds_read_b128 a[88:91], v202 offset:16640\n"
    "v_mfma_f32_16x16x32_bf16 v[44:47], v[188:191], a[148:151], v[44:47]\n"
    "v_exp_f32 v179, v131\n"
    "v_mfma_f32_16x16x32_bf16 v[40:43], v[184:187], a[148:151], v[40:43]\n"
    "ds_read_b128 a[92:95], v203 offset:16640\n"
    "v_add_f32 v222, v222, v179\n"
    "v_cvt_pk_bf16_f32 v233, v178, v179\n"
    "v_mfma_f32_16x16x32_bf16 v[48:51], v[184:187], a[152:155], v[48:51]\n"
    "v_exp_f32 v180, v136\n"
    "v_mfma_f32_16x16x32_bf16 v[52:55], v[188:191], a[152:155], v[52:55]\n"
    "buffer_load_dwordx4 v218, s[28:31], s44 offen offset:2048 nt lds\n"
    "ds_read_b128 a[96:99], v200 offset:24576\n"
    "v_mfma_f32_16x16x32_bf16 v[60:63], v[188:191], a[156:159], v[60:63]\n"
    "v_exp_f32 v181, v137\n"
    "v_mfma_f32_16x16x32_bf16 v[56:59], v[184:187], a[156:159], v[56:59]\n"
    "s_waitcnt lgkmcnt(5)\n"
    "ds_read_b128 a[100:103], v201 offset:24576\n"
    "v_add_f32 v223, v180, v181\n"
    "v_mfma_f32_16x16x32_bf16 v[144:147], a[64:67], a[0:3], v[192:195]\n"
    "ds_read_b128 a[104:107], v202 offset:24576\n"
    "v_cvt_pk_bf16_f32 v236, v180, v181\n"
    "v_mfma_f32_16x16x32_bf16 v[144:147], a[68:71], a[4:7], v[144:147]\n"
    "v_exp_f32 v182, v138\n"
    "v_mfma_f32_16x16x32_bf16 v[144:147], a[72:75], a[8:11], v[144:147]\n"
    "buffer_load_dwordx4 v219, s[28:31], s44 offen offset:3072 nt lds\n"
    "v_add_f32 v223, v223, v182\n"
    "v_mfma_f32_16x16x32_bf16 v[144:147], a[76:79], a[12:15], v[144:147]\n"
    "s_waitcnt lgkmcnt(3)\n"
    "v_exp_f32 v183, v139\n"
    "v_mfma_f32_16x16x32_bf16 v[144:147], a[80:83], a[16:19], v[144:147]\n"
    "ds_read_b128 a[108:111], v203 offset:24576\n"
    "v_add_f32 v223, v223, v183\n"
    "v_mfma_f32_16x16x32_bf16 v[144:147], a[84:87], a[20:23], v[144:147]\n"
    "ds_read_b128 a[112:115], v200 offset:24832\n"
    "v_cvt_pk_bf16_f32 v237, v182, v183\n"
    "v_mfma_f32_16x16x32_bf16 v[144:147], a[88:91], a[24:27], v[144:147]\n"
    "v_exp_f32 v176, v132\n"
    "v_mfma_f32_16x16x32_bf16 v[144:147], a[92:95], a[28:31], v[144:147]\n"
    "ds_read_b128 a[116:119], v201 offset:24832\n"
    "v_add_f32 v222, v222, v176\n"
    "v_mfma_f32_16x16x32_bf16 v[152:155], a[92:95], a[60:63], v[196:199]\n"
    "s_add_u32 m0, s45, 49152\n"
    "v_exp_f32 v177, v133\n"
    "v_mfma_f32_16x16x32_bf16 v[152:155], a[88:91], a[56:59], v[152:155]\n"
    "buffer_load_dwordx4 v212, s[24:27], s44 offen offset:0 nt lds\n"
    "ds_read_b128 a[120:123], v202 offset:24832\n"
    "v_add_f32 v222, v222, v177\n"
    "v_mfma_f32_16x16x32_bf16 v[152:155], a[84:87], a[52:55], v[152:155]\n"
    "ds_read_b128 a[124:127], v203 offset:24832\n"
    "v_cvt_pk_bf16_f32 v234, v176, v177\n"
    "v_mfma_f32_16x16x32_bf16 v[152:155], a[80:83], a[48:51], v[152:155]\n"
    "v_exp_f32 v178, v134\n"
    "v_mfma_f32_16x16x32_bf16 v[152:155], a[76:79], a[44:47], v[152:155]\n"
    "ds_read_b64_tr_b16 a[128:129], v204 offset:0\n"
    "v_add_f32 v222, v222, v178\n"
    "v_mfma_f32_16x16x32_bf16 v[152:155], a[72:75], a[40:43], v[152:155]\n"
    "v_exp_f32 v179, v135\n"
    "v_mfma_f32_16x16x32_bf16 v[152:155], a[68:71], a[36:39], v[152:155]\n"
    "buffer_load_dwordx4 v213, s[24:27], s44 offen offset:1024 nt lds\n"
    "v_add_f32 v222, v222, v179\n"
    "v_cvt_pk_bf16_f32 v235, v178, v179\n"
    "v_mfma_f32_16x16x32_bf16 v[152:155], a[64:67], a[32:35], v[152:155]\n"
    "s_waitcnt lgkmcnt(1)\n"
    "v_exp_f32 v180, v140\n"
    "v_mfma_f32_16x16x32_bf16 v[156:159], a[96:99], a[32:35], v[196:199]\n"
    "ds_read_b64_tr_b16 a[130:131], v204 offset:8192\n"
    "ds_read_b64_tr_b16 a[132:133], v205 offset:0\n"
    "v_add_f32 v223, v223, v180\n"
    "v_mfma_f32_16x16x32_bf16 v[156:159], a[100:103], a[36:39], v[156:159]\n"
    "v_exp_f32 v181, v141\n"
    "v_mfma_f32_16x16x32_bf16 v[156:159], a[104:107], a[40:43], v[156:159]\n"
    "ds_read_b64_tr_b16 a[134:135], v205 offset:8192\n"
    "v_add_f32 v223, v223, v181\n"
    "v_cvt_pk_bf16_f32 v238, v180, v181\n"
    "v_mfma_f32_16x16x32_bf16 v[156:159], a[108:111], a[44:47], v[156:159]\n"
    "v_exp_f32 v182, v142\n"
    "v_mfma_f32_16x16x32_bf16 v[156:159], a[112:115], a[48:51], v[156:159]\n"
    "buffer_load_dwordx4 v214, s[24:27], s44 offen offset:2048 nt lds\n"
    "v_add_f32 v223, v223, v182\n"
    "v_mfma_f32_16x16x32_bf16 v[156:159], a[116:119], a[52:55], v[156:159]\n"
    "v_exp_f32 v183, v143\n"
    "v_mfma_f32_16x16x32_bf16 v[156:159], a[120:123], a[56:59], v[156:159]\n"
    "v_add_f32 v223, v223, v183\n"
    "v_add_f32 v224, v222, v223\n"
    "v_mfma_f32_16x16x32_bf16 v[156:159], a[124:127], a[60:63], v[156:159]\n"
    "v_cmp_nge_f32 vcc, s47, v224\n"
    "v_cvt_pk_bf16_f32 v239, v182, v183\n"
    "v_mfma_f32_16x16x32_bf16 v[148:151], a[124:127], a[28:31], v[192:195]\n"
    "ds_read_b64_tr_b16 a[136:137], v206 offset:0\n"
    "ds_read_b64_tr_b16 a[138:139], v206 offset:8192\n"
    "v_mfma_f32_16x16x32_bf16 v[148:151], a[120:123], a[24:27], v[148:151]\n"
    "buffer_load_dwordx4 v215, s[24:27], s44 offen offset:3072 nt lds\n"
    "ds_read_b64_tr_b16 a[140:141], v207 offset:0\n"
    "ds_read_b64_tr_b16 a[142:143], v207 offset:8192\n"
    "v_mfma_f32_16x16x32_bf16 v[148:151], a[116:119], a[20:23], v[148:151]\n"
    "s_cbranch_vccnz .Lstub_q28_%=\n"
    ".Lret_q28_%=:\n"
    "v_mfma_f32_16x16x32_bf16 v[148:151], a[112:115], a[16:19], v[148:151]\n"
    "s_add_u32 s44, s44, 16384\n"
    "ds_read_b64_tr_b16 a[144:145], v208 offset:0\n"
    "ds_read_b64_tr_b16 a[146:147], v208 offset:8192\n"
    "v_mfma_f32_16x16x32_bf16 v[148:151], a[108:111], a[12:15], v[148:151]\n"
    "s_add_u32 m0, s45, 147456\n"
    "ds_read_b64_tr_b16 a[148:149], v209 offset:0\n"
    "ds_read_b64_tr_b16 a[150:151], v209 offset:8192\n"
    "v_mfma_f32_16x16x32_bf16 v[148:151], a[104:107], a[8:11], v[148:151]\n"
    "ds_read_b64_tr_b16 a[152:153], v210 offset:0\n"
    "ds_read_b64_tr_b16 a[154:155], v210 offset:8192\n"
    "v_mfma_f32_16x16x32_bf16 v[148:151], a[100:103], a[4:7], v[148:151]\n"
    "ds_read_b64_tr_b16 a[156:157], v211 offset:0\n"
    "ds_read_b64_tr_b16 a[158:159], v211 offset:8192\n"
    "v_mfma_f32_16x16x32_bf16 v[148:151], a[96:99], a[0:3], v[148:151]\n"
    "s_waitcnt vmcnt(8) lgkmcnt(10)\n"
    "s_barrier\n"
    "v_mfma_f32_16x16x32_bf16 v[0:3], v[232:235], a[128:131], v[0:3]\n"
    "ds_read_b128 a[64:67], v200 offset:32768\n"
    "v_add_f32 v220, v220, v222\n"
    "v_mfma_f32_16x16x32_bf16 v[4:7], v[236:239], a[128:131], v[4:7]\n"
    "ds_read_b128 a[68:71], v201 offset:32768\n"
    "v_add_f32 v221, v221, v223\n"
    "v_mfma_f32_16x16x32_bf16 v[12:15], v[236:239], a[132:135], v[12:15]\n"
    "v_exp_f32 v176, v144\n"
    "v_mfma_f32_16x16x32_bf16 v[8:11], v[232:235], a[132:135], v[8:11]\n"
    "buffer_load_dwordx4 v216, s[28:31], s44 offen offset:0 nt lds\n"
    "ds_read_b128 a[72:75], v202 offset:32768\n"
    "v_mfma_f32_16x16x32_bf16 v[16:19], v[232:235], a[136:139], v[16:19]\n"
    "v_exp_f32 v177, v145\n"
    "v_mfma_f32_16x16x32_bf16 v[20:23], v[236:239], a[136:139], v[20:23]\n"
    "s_waitcnt lgkmcnt(3)\n"
    "ds_read_b128 a[76:79], v203 offset:32768\n"
    "v_add_f32 v222, v176, v177\n"
    "v_mfma_f32_16x16x32_bf16 v[28:31], v[236:239], a[140:143], v[28:31]\n"
    "ds_read_b128 a[80:83], v200 offset:33024\n"
    "v_cvt_pk_bf16_f32 v240, v176, v177\n"
    "v_mfma_f32_16x16x32_bf16 v[24:27], v[232:235], a[140:143], v[24:27]\n"
    "v_exp_f32 v178, v146\n"
    "v_mfma_f32_16x16x32_bf16 v[32:35], v[232:235], a[144:147], v[32:35]\n"
    "ds_read_b128 a[84:87], v201 offset:33024\n"
    "v_add_f32 v222, v222, v178\n"
    "v_mfma_f32_16x16x32_bf16 v[36:39], v[236:239], a[144:147], v[36:39]\n"
    "buffer_load_dwordx4 v217, s[28:31], s44 offen offset:1024 nt lds\n"
    "ds_read_b128 a[88:91], v202 offset:33024\n"
    "v_mfma_f32_16x16x32_bf16 v[44:47], v[236:239], a[148:151], v[44:47]\n"
    "v_exp_f32 v179, v147\n"
    "v_mfma_f32_16x16x32_bf16 v[40:43], v[232:235], a[148:151], v[40:43]\n"
    "ds_read_b128 a[92:95], v203 offset:33024\n"
    "v_add_f32 v222, v222, v179\n"
    "v_cvt_pk_bf16_f32 v241, v178, v179\n"
    "v_mfma_f32_16x16x32_bf16 v[48:51], v[232:235], a[152:155], v[48:51]\n"
    "v_exp_f32 v180, v152\n"
    "v_mfma_f32_16x16x32_bf16 v[52:55], v[236:239], a[152:155], v[52:55]\n"
    "buffer_load_dwordx4 v218, s[28:31], s44 offen offset:2048 nt lds\n"
    "ds_read_b128 a[96:99], v200 offset:40960\n"
    "v_mfma_f32_16x16x32_bf16 v[60:63], v[236:239], a[156:159], v[60:63]\n"
    "v_exp_f32 v181, v153\n"
    "v_mfma_f32_16x16x32_bf16 v[56:59], v[232:235], a[156:159], v[56:59]\n"
    "s_waitcnt lgkmcnt(5)\n"
    "ds_read_b128 a[100:103], v201 offset:40960\n"
    "v_add_f32 v223, v180, v181\n"
    "v_mfma_f32_16x16x32_bf16 v[128:131], a[64:67], a[0:3], v[192:195]\n"
    "ds_read_b128 a[104:107], v202 offset:40960\n"
    "v_cvt_pk_bf16_f32 v244, v180, v181\n"
    "v_mfma_f32_16x16x32_bf16 v[128:131], a[68:71], a[4:7], v[128:131]\n"
    "v_exp_f32 v182, v154\n"
    "v_mfma_f32_16x16x32_bf16 v[128:131], a[72:75], a[8:11], v[128:131]\n"
    "buffer_load_dwordx4 v219, s[28:31], s44 offen offset:3072 nt lds\n"
    "v_add_f32 v223, v223, v182\n"
    "v_mfma_f32_16x16x32_bf16 v[128:131], a[76:79], a[12:15], v[128:131]\n"
    "s_waitcnt lgkmcnt(3)\n"
    "v_exp_f32 v183, v155\n"
    "v_mfma_f32_16x16x32_bf16 v[128:131], a[80:83], a[16:19], v[128:131]\n"
    "ds_read_b128 a[108:111], v203 offset:40960\n"
    "v_add_f32 v223, v223, v183\n"
    "v_mfma_f32_16x16x32_bf16 v[128:131], a[84:87], a[20:23], v[128:131]\n"
    "ds_read_b128 a[112:115], v200 offset:41216\n"
    "v_cvt_pk_bf16_f32 v245, v182, v183\n"
    "v_mfma_f32_16x16x32_bf16 v[128:131], a[88:91], a[24:27], v[128:131]\n"
    "v_exp_f32 v176, v148\n"
    "v_mfma_f32_16x16x32_bf16 v[128:131], a[92:95], a[28:31], v[128:131]\n"
    "ds_read_b128 a[116:119], v201 offset:41216\n"
    "v_add_f32 v222, v222, v176\n"
    "v_mfma_f32_16x16x32_bf16 v[136:139], a[92:95], a[60:63], v[196:199]\n"
    "v_exp_f32 v177, v149\n"
    "v_mfma_f32_16x16x32_bf16 v[136:139], a[88:91], a[56:59], v[136:139]\n"
    "ds_read_b128 a[120:123], v202 offset:41216\n"
    "v_add_f32 v222, v222, v177\n"
    "v_and_b32 v252, 16, v225\n"
    "v_mfma_f32_16x16x32_bf16 v[136:139], a[84:87], a[52:55], v[136:139]\n"
    "ds_read_b128 a[124:127], v203 offset:41216\n"
    "v_cvt_pk_bf16_f32 v242, v176, v177\n"
    "v_mfma_f32_16x16x32_bf16 v[136:139], a[80:83], a[48:51], v[136:139]\n"
    "v_exp_f32 v178, v150\n"
    "v_mfma_f32_16x16x32_bf16 v[136:139], a[76:79], a[44:47], v[136:139]\n"
    "ds_read_b64_tr_b16 a[128:129], v204 offset:16384\n"
    "v_add_f32 v222, v222, v178\n"
    "v_mfma_f32_16x16x32_bf16 v[136:139], a[72:75], a[40:43], v[136:139]\n"
    "buffer_load_dword v248, off, s[16:19], 0\n"
    "v_exp_f32 v179, v151\n"
    "v_mfma_f32_16x16x32_bf16 v[136:139], a[68:71], a[36:39], v[136:139]\n"
    "v_add_f32 v222, v222, v179\n"
    "v_cvt_pk_bf16_f32 v243, v178, v179\n"
    "v_lshl_or_b32 v252, v252, 12, v226\n"
    "v_mfma_f32_16x16x32_bf16 v[136:139], a[64:67], a[32:35], v[136:139]\n"
    "s_waitcnt lgkmcnt(1)\n"
    "v_exp_f32 v180, v156\n"
    "v_mfma_f32_16x16x32_bf16 v[140:143], a[96:99], a[32:35], v[196:199]\n"
    "ds_read_b64_tr_b16 a[130:131], v204 offset:24576\n"
    "ds_read_b64_tr_b16 a[132:133], v205 offset:16384\n"
    "v_add_f32 v223, v223, v180\n"
    "v_mfma_f32_16x16x32_bf16 v[140:143], a[100:103], a[36:39], v[140:143]\n"
    "v_exp_f32 v181, v157\n"
    "v_mfma_f32_16x16x32_bf16 v[140:143], a[104:107], a[40:43], v[140:143]\n"
    "ds_read_b64_tr_b16 a[134:135], v205 offset:24576\n"
    "v_add_f32 v223, v223, v181\n"
    "v_cvt_pk_bf16_f32 v246, v180, v181\n"
    "v_mfma_f32_16x16x32_bf16 v[140:143], a[108:111], a[44:47], v[140:143]\n"
    "v_exp_f32 v182, v158\n"
    "v_mfma_f32_16x16x32_bf16 v[140:143], a[112:115], a[48:51], v[140:143]\n"
    "buffer_load_dword v249, off, s[40:43], s58 offset:12 sc1\n"
    "v_add_f32 v223, v223, v182\n"
    "v_mfma_f32_16x16x32_bf16 v[140:143], a[116:119], a[52:55], v[140:143]\n"
    "v_exp_f32 v183, v159\n"
    "v_mfma_f32_16x16x32_bf16 v[140:143], a[120:123], a[56:59], v[140:143]\n"
    "v_add_f32 v223, v223, v183\n"
    "v_add_f32 v224, v222, v223\n"
    "v_mfma_f32_16x16x32_bf16 v[140:143], a[124:127], a[60:63], v[140:143]\n"
    "v_cmp_nge_f32 vcc, s47, v224\n"
    "v_cvt_pk_bf16_f32 v247, v182, v183\n"
    "v_mfma_f32_16x16x32_bf16 v[132:135], a[124:127], a[28:31], v[192:195]\n"
    "ds_read_b64_tr_b16 a[136:137], v206 offset:16384\n"
    "ds_read_b64_tr_b16 a[138:139], v206 offset:24576\n"
    "v_mfma_f32_16x16x32_bf16 v[132:135], a[120:123], a[24:27], v[132:135]\n"
    "ds_read_b64_tr_b16 a[140:141], v207 offset:16384\n"
    "ds_read_b64_tr_b16 a[142:143], v207 offset:24576\n"
    "v_lshlrev_b32 v227, 4, v225\n"
    "v_mfma_f32_16x16x32_bf16 v[132:135], a[116:119], a[20:23], v[132:135]\n"
    "s_cbranch_vccnz .Lstub_q29_%=\n"
    ".Lret_q29_%=:\n"
    "v_mfma_f32_16x16x32_bf16 v[132:135], a[112:115], a[16:19], v[132:135]\n"
    "ds_read_b64_tr_b16 a[144:145], v208 offset:16384\n"
    "ds_read_b64_tr_b16 a[146:147], v208 offset:24576\n"
    "v_min_u32 v255, 2, v226\n"
    "v_mfma_f32_16x16x32_bf16 v[132:135], a[108:111], a[12:15], v[132:135]\n"
    "ds_read_b64_tr_b16 a[148:149], v209 offset:16384\n"
    "ds_read_b64_tr_b16 a[150:151], v209 offset:24576\n"
    "v_xor_b32 v255, 2, v255\n"
    "v_mfma_f32_16x16x32_bf16 v[132:135], a[104:107], a[8:11], v[132:135]\n"
    "ds_read_b64_tr_b16 a[152:153], v210 offset:16384\n"
    "ds_read_b64_tr_b16 a[154:155], v210 offset:24576\n"
    "v_mfma_f32_16x16x32_bf16 v[132:135], a[100:103], a[4:7], v[132:135]\n"
    "ds_read_b64_tr_b16 a[156:157], v211 offset:16384\n"
    "ds_read_b64_tr_b16 a[158:159], v211 offset:24576\n"
    "v_mfma_f32_16x16x32_bf16 v[132:135], a[96:99], a[0:3], v[132:135]\n"
    "s_waitcnt vmcnt(6) lgkmcnt(10)\n"
    "s_barrier\n"
    "v_mfma_f32_16x16x32_bf16 v[0:3], v[240:243], a[128:131], v[0:3]\n"
    "ds_read_b128 a[64:67], v200 offset:49152\n"
    "v_add_f32 v220, v220, v222\n"
    "v_mfma_f32_16x16x32_bf16 v[4:7], v[244:247], a[128:131], v[4:7]\n"
    "ds_read_b128 a[68:71], v201 offset:49152\n"
    "v_add_f32 v221, v221, v223\n"
    "v_mfma_f32_16x16x32_bf16 v[12:15], v[244:247], a[132:135], v[12:15]\n"
    "v_exp_f32 v176, v128\n"
    "v_mfma_f32_16x16x32_bf16 v[8:11], v[240:243], a[132:135], v[8:11]\n"
    "ds_read_b128 a[72:75], v202 offset:49152\n"
    "v_add_u32 v204, 32768, v204\n"
    "v_mfma_f32_16x16x32_bf16 v[16:19], v[240:243], a[136:139], v[16:19]\n"
    "v_exp_f32 v177, v129\n"
    "v_mfma_f32_16x16x32_bf16 v[20:23], v[244:247], a[136:139], v[20:23]\n"
    "s_waitcnt lgkmcnt(3)\n"
    "ds_read_b128 a[76:79], v203 offset:49152\n"
    "v_add_f32 v222, v176, v177\n"
    "v_mfma_f32_16x16x32_bf16 v[28:31], v[244:247], a[140:143], v[28:31]\n"
    "ds_read_b128 a[80:83], v200 offset:49408\n"
    "v_cvt_pk_bf16_f32 v212, v176, v177\n"
    "v_mfma_f32_16x16x32_bf16 v[24:27], v[240:243], a[140:143], v[24:27]\n"
    "v_exp_f32 v178, v130\n"
    "v_mfma_f32_16x16x32_bf16 v[32:35], v[240:243], a[144:147], v[32:35]\n"
    "ds_read_b128 a[84:87], v201 offset:49408\n"
    "v_add_f32 v222, v222, v178\n"
    "v_mfma_f32_16x16x32_bf16 v[36:39], v[244:247], a[144:147], v[36:39]\n"
    "ds_read_b128 a[88:91], v202 offset:49408\n"
    "v_add_u32 v205, 32768, v205\n"
    "v_mfma_f32_16x16x32_bf16 v[44:47], v[244:247], a[148:151], v[44:47]\n"
    "v_exp_f32 v179, v131\n"
    "v_mfma_f32_16x16x32_bf16 v[40:43], v[240:243], a[148:151], v[40:43]\n"
    "ds_read_b128 a[92:95], v203 offset:49408\n"
    "v_add_f32 v222, v222, v179\n"
    "v_cvt_pk_bf16_f32 v213, v178, v179\n"
    "v_mfma_f32_16x16x32_bf16 v[48:51], v[240:243], a[152:155], v[48:51]\n"
    "v_exp_f32 v180, v136\n"
    "v_mfma_f32_16x16x32_bf16 v[52:55], v[244:247], a[152:155], v[52:55]\n"
    "ds_read_b128 a[96:99], v200 offset:57344\n"
    "v_add_u32 v206, 32768, v206\n"
    "v_mfma_f32_16x16x32_bf16 v[60:63], v[244:247], a[156:159], v[60:63]\n"
    "v_exp_f32 v181, v137\n"
    "v_mfma_f32_16x16x32_bf16 v[56:59], v[240:243], a[156:159], v[56:59]\n"
    "s_waitcnt lgkmcnt(5)\n"
    "ds_read_b128 a[100:103], v201 offset:57344\n"
    "v_add_f32 v223, v180, v181\n"
    "v_mfma_f32_16x16x32_bf16 v[144:147], a[64:67], a[0:3], v[192:195]\n"
    "ds_read_b128 a[104:107], v202 offset:57344\n"
    "v_cvt_pk_bf16_f32 v216, v180, v181\n"
    "v_mfma_f32_16x16x32_bf16 v[144:147], a[68:71], a[4:7], v[144:147]\n"
    "v_exp_f32 v182, v138\n"
    "v_mfma_f32_16x16x32_bf16 v[144:147], a[72:75], a[8:11], v[144:147]\n"
    "v_add_u32 v207, 32768, v207\n"
    "v_add_f32 v223, v223, v182\n"
    "v_mfma_f32_16x16x32_bf16 v[144:147], a[76:79], a[12:15], v[144:147]\n"
    "s_waitcnt lgkmcnt(3)\n"
    "v_exp_f32 v183, v139\n"
    "v_mfma_f32_16x16x32_bf16 v[144:147], a[80:83], a[16:19], v[144:147]\n"
    "ds_read_b128 a[108:111], v203 offset:57344\n"
    "v_add_f32 v223, v223, v183\n"
    "v_mfma_f32_16x16x32_bf16 v[144:147], a[84:87], a[20:23], v[144:147]\n"
    "ds_read_b128 a[112:115], v200 offset:57600\n"
    "v_cvt_pk_bf16_f32 v217, v182, v183\n"
    "v_mfma_f32_16x16x32_bf16 v[144:147], a[88:91], a[24:27], v[144:147]\n"
    "v_exp_f32 v176, v132\n"
    "v_mfma_f32_16x16x32_bf16 v[144:147], a[92:95], a[28:31], v[144:147]\n"
    "ds_read_b128 a[116:119], v201 offset:57600\n"
    "v_add_f32 v222, v222, v176\n"
    "v_mfma_f32_16x16x32_bf16 v[152:155], a[92:95], a[60:63], v[196:199]\n"
    "v_exp_f32 v177, v133\n"
    "v_mfma_f32_16x16x32_bf16 v[152:155], a[88:91], a[56:59], v[152:155]\n"
    "ds_read_b128 a[120:123], v202 offset:57600\n"
    "v_add_u32 v208, 32768, v208\n"
    "v_add_f32 v222, v222, v177\n"
    "v_mfma_f32_16x16x32_bf16 v[152:155], a[84:87], a[52:55], v[152:155]\n"
    "ds_read_b128 a[124:127], v203 offset:57600\n"
    "v_cvt_pk_bf16_f32 v214, v176, v177\n"
    "v_mfma_f32_16x16x32_bf16 v[152:155], a[80:83], a[48:51], v[152:155]\n"
    "v_exp_f32 v178, v134\n"
    "v_mfma_f32_16x16x32_bf16 v[152:155], a[76:79], a[44:47], v[152:155]\n"
    "ds_read_b64_tr_b16 a[128:129], v204 offset:32768\n"
    "v_add_f32 v222, v222, v178\n"
    "v_mfma_f32_16x16x32_bf16 v[152:155], a[72:75], a[40:43], v[152:155]\n"
    "v_exp_f32 v179, v135\n"
    "v_mfma_f32_16x16x32_bf16 v[152:155], a[68:71], a[36:39], v[152:155]\n"
    "v_add_u32 v209, 32768, v209\n"
    "v_add_f32 v222, v222, v179\n"
    "v_cvt_pk_bf16_f32 v215, v178, v179\n"
    "v_mfma_f32_16x16x32_bf16 v[152:155], a[64:67], a[32:35], v[152:155]\n"
    "s_waitcnt lgkmcnt(1)\n"
    "v_exp_f32 v180, v140\n"
    "v_mfma_f32_16x16x32_bf16 v[156:159], a[96:99], a[32:35], v[196:199]\n"
    "ds_read_b64_tr_b16 a[130:131], v204 offset:40960\n"
    "ds_read_b64_tr_b16 a[132:133], v205 offset:32768\n"
    "v_add_f32 v223, v223, v180\n"
    "v_mfma_f32_16x16x32_bf16 v[156:159], a[100:103], a[36:39], v[156:159]\n"
    "v_exp_f32 v181, v141\n"
    "v_mfma_f32_16x16x32_bf16 v[156:159], a[104:107], a[40:43], v[156:159]\n"
    "ds_read_b64_tr_b16 a[134:135], v205 offset:40960\n"
    "v_add_f32 v223, v223, v181\n"
    "v_cvt_pk_bf16_f32 v218, v180, v181\n"
    "v_mfma_f32_16x16x32_bf16 v[156:159], a[108:111], a[44:47], v[156:159]\n"
    "v_exp_f32 v182, v142\n"
    "v_mfma_f32_16x16x32_bf16 v[156:159], a[112:115], a[48:51], v[156:159]\n"
    "v_add_u32 v210, 32768, v210\n"
    "v_add_f32 v223, v223, v182\n"
    "v_mfma_f32_16x16x32_bf16 v[156:159], a[116:119], a[52:55], v[156:159]\n"
    "v_exp_f32 v183, v143\n"
    "v_mfma_f32_16x16x32_bf16 v[156:159], a[120:123], a[56:59], v[156:159]\n"
    "v_add_f32 v223, v223, v183\n"
    "v_add_f32 v224, v222, v223\n"
    "v_mfma_f32_16x16x32_bf16 v[156:159], a[124:127], a[60:63], v[156:159]\n"
    "v_cmp_nge_f32 vcc, s47, v224\n"
    "v_cvt_pk_bf16_f32 v219, v182, v183\n"
    "v_mfma_f32_16x16x32_bf16 v[148:151], a[124:127], a[28:31], v[192:195]\n"
    "ds_read_b64_tr_b16 a[136:137], v206 offset:32768\n"
    "ds_read_b64_tr_b16 a[138:139], v206 offset:40960\n"
    "v_mfma_f32_16x16x32_bf16 v[148:151], a[120:123], a[24:27], v[148:151]\n"
    "ds_read_b64_tr_b16 a[140:141], v207 offset:32768\n"
    "ds_read_b64_tr_b16 a[142:143], v207 offset:40960\n"
    "v_add_u32 v211, 32768, v211\n"
    "v_mfma_f32_16x16x32_bf16 v[148:151], a[116:119], a[20:23], v[148:151]\n"
    "s_cbranch_vccnz .Lstub_q30_%=\n"
    ".Lret_q30_%=:\n"
    "v_mfma_f32_16x16x32_bf16 v[148:151], a[112:115], a[16:19], v[148:151]\n"
    "ds_read_b64_tr_b16 a[144:145], v208 offset:32768\n"
    "ds_read_b64_tr_b16 a[146:147], v208 offset:40960\n"
    "v_mfma_f32_16x16x32_bf16 v[148:151], a[108:111], a[12:15], v[148:151]\n"
    "ds_read_b64_tr_b16 a[148:149], v209 offset:32768\n"
    "ds_read_b64_tr_b16 a[150:151], v209 offset:40960\n"
    "v_mfma_f32_16x16x32_bf16 v[148:151], a[104:107], a[8:11], v[148:151]\n"
    "ds_read_b64_tr_b16 a[152:153], v210 offset:32768\n"
    "ds_read_b64_tr_b16 a[154:155], v210 offset:40960\n"
    "v_mfma_f32_16x16x32_bf16 v[148:151], a[100:103], a[4:7], v[148:151]\n"
    "ds_read_b64_tr_b16 a[156:157], v211 offset:32768\n"
    "ds_read_b64_tr_b16 a[158:159], v211 offset:40960\n"
    "v_mfma_f32_16x16x32_bf16 v[148:151], a[96:99], a[0:3], v[148:151]\n"
    "s_waitcnt vmcnt(2) lgkmcnt(10)\n"
    "s_barrier\n"
    "v_mfma_f32_16x16x32_bf16 v[0:3], v[212:215], a[128:131], v[0:3]\n"
    "v_add_f32 v220, v220, v222\n"
    "v_add_f32 v221, v221, v223\n"
    "v_exp_f32 v176, v144\n"
    "v_exp_f32 v177, v145\n"
    "v_mfma_f32_16x16x32_bf16 v[4:7], v[216:219], a[128:131], v[4:7]\n"
    "ds_read_b64_tr_b16 a[128:129], v204 offset:49152\n"
    "ds_read_b64_tr_b16 a[130:131], v204 offset:57344\n"
    "v_exp_f32 v178, v146\n"
    "v_add_f32 v222, v176, v177\n"
    "v_cvt_pk_bf16_f32 v128, v176, v177\n"
    "v_mfma_f32_16x16x32_bf16 v[12:15], v[216:219], a[132:135], v[12:15]\n"
    "v_exp_f32 v179, v147\n"
    "v_add_f32 v222, v222, v178\n"
    "v_exp_f32 v180, v152\n"
    "v_mfma_f32_16x16x32_bf16 v[8:11], v[212:215], a[132:135], v[8:11]\n"
    "ds_read_b64_tr_b16 a[132:133], v205 offset:49152\n"
    "ds_read_b64_tr_b16 a[134:135], v205 offset:57344\n"
    "v_add_f32 v222, v222, v179\n"
    "v_cvt_pk_bf16_f32 v129, v178, v179\n"
    "v_exp_f32 v181, v153\n"
    "v_mfma_f32_16x16x32_bf16 v[16:19], v[212:215], a[136:139], v[16:19]\n"
    "v_exp_f32 v182, v154\n"
    "v_add_f32 v223, v180, v181\n"
    "v_cvt_pk_bf16_f32 v132, v180, v181\n"
    "v_mfma_f32_16x16x32_bf16 v[20:23], v[216:219], a[136:139], v[20:23]\n"
    "s_waitcnt lgkmcnt(4)\n"
    "ds_read_b64_tr_b16 a[136:137], v206 offset:49152\n"
    "ds_read_b64_tr_b16 a[138:139], v206 offset:57344\n"
    "v_exp_f32 v183, v155\n"
    "v_add_f32 v223, v223, v182\n"
    "v_exp_f32 v176, v148\n"
    "v_mfma_f32_16x16x32_bf16 v[28:31], v[216:219], a[140:143], v[28:31]\n"
    "v_add_f32 v223, v223, v183\n"
    "v_cvt_pk_bf16_f32 v133, v182, v183\n"
    "v_exp_f32 v177, v149\n"
    "v_mfma_f32_16x16x32_bf16 v[24:27], v[212:215], a[140:143], v[24:27]\n"
    "ds_read_b64_tr_b16 a[140:141], v207 offset:49152\n"
    "ds_read_b64_tr_b16 a[142:143], v207 offset:57344\n"
    "v_add_f32 v222, v222, v176\n"
    "v_exp_f32 v178, v150\n"
    "v_add_f32 v222, v222, v177\n"
    "v_mfma_f32_16x16x32_bf16 v[32:35], v[212:215], a[144:147], v[32:35]\n"
    "v_cvt_pk_bf16_f32 v130, v176, v177\n"
    "v_exp_f32 v179, v151\n"
    "v_add_f32 v222, v222, v178\n"
    "v_mfma_f32_16x16x32_bf16 v[36:39], v[216:219], a[144:147], v[36:39]\n"
    "ds_read_b64_tr_b16 a[144:145], v208 offset:49152\n"
    "ds_read_b64_tr_b16 a[146:147], v208 offset:57344\n"
    "v_exp_f32 v180, v156\n"
    "v_add_f32 v222, v222, v179\n"
    "v_cvt_pk_bf16_f32 v131, v178, v179\n"
    "v_mfma_f32_16x16x32_bf16 v[44:47], v[216:219], a[148:151], v[44:47]\n"
    "v_exp_f32 v181, v157\n"
    "v_add_f32 v223, v223, v180\n"
    "v_exp_f32 v182, v158\n"
    "v_mfma_f32_16x16x32_bf16 v[40:43], v[212:215], a[148:151], v[40:43]\n"
    "ds_read_b64_tr_b16 a[148:149], v209 offset:49152\n"
    "ds_read_b64_tr_b16 a[150:151], v209 offset:57344\n"
    "v_add_f32 v223, v223, v181\n"
    "v_cvt_pk_bf16_f32 v134, v180, v181\n"
    "v_exp_f32 v183, v159\n"
    "v_mfma_f32_16x16x32_bf16 v[48:51], v[212:215], a[152:155], v[48:51]\n"
    "v_add_f32 v223, v223, v182\n"
    "s_mov_b32 s55, 4096\n"
    "v_add_f32 v223, v223, v183\n"
    "v_mfma_f32_16x16x32_bf16 v[52:55], v[216:219], a[152:155], v[52:55]\n"
    "ds_read_b64_tr_b16 a[152:153], v210 offset:49152\n"
    "ds_read_b64_tr_b16 a[154:155], v210 offset:57344\n"
    "v_add_f32 v224, v222, v223\n"
    "v_cmp_nge_f32 vcc, s47, v224\n"
    "v_cvt_pk_bf16_f32 v135, v182, v183\n"
    "v_mfma_f32_16x16x32_bf16 v[60:63], v[216:219], a[156:159], v[60:63]\n"
    "s_waitcnt vmcnt(0)\n"
    "v_bfi_b32 v230, v255, 0, v249\n"
    "v_mfma_f32_16x16x32_bf16 v[56:59], v[212:215], a[156:159], v[56:59]\n"
    "s_cbranch_vccnz .Lstub_bl_%=\n"
    ".Lret_bl_%=:\n"
    "ds_read_b64_tr_b16 a[156:157], v211 offset:49152\n"
    "ds_read_b64_tr_b16 a[158:159], v211 offset:57344\n"
    "v_add_f32 v220, v220, v222\n"
    "v_add_f32 v221, v221, v223\n"
    "s_waitcnt lgkmcnt(8)\n"
    "v_mfma_f32_16x16x32_bf16 v[0:3], v[128:131], a[128:131], v[0:3]\n"
    "v_permlane32_swap_b32 v193, v197\n"
    "v_permlane32_swap_b32 v220, v221\n"
    "v_mfma_f32_16x16x32_bf16 v[8:11], v[128:131], a[132:135], v[8:11]\n"
    "v_add_f32 v220, v220, v221\n"
    "v_mov_b32 v221, v220\n"
    "v_mfma_f32_16x16x32_bf16 v[16:19], v[128:131], a[136:139], v[16:19]\n"
    "v_mov_b32 v229, v193\n"
    "v_permlane16_swap_b32 v220, v221\n"
    "v_mfma_f32_16x16x32_bf16 v[24:27], v[128:131], a[140:143], v[24:27]\n"
    "s_waitcnt lgkmcnt(0)\n"
    "v_add_f32 v228, v220, v221\n"
    "v_mfma_f32_16x16x32_bf16 v[32:35], v[128:131], a[144:147], v[32:35]\n"
    "buffer_store_dwordx4 v[228:231], v252, s[36:39], s58 offen sc1\n"
    "v_cvt_pk_bf16_f32 v0, v0, v1\n"
    "v_cvt_pk_bf16_f32 v1, v2, v3\n"
    "v_mfma_f32_16x16x32_bf16 v[40:43], v[128:131], a[148:151], v[40:43]\n"
    "v_cvt_pk_bf16_f32 v2, v8, v9\n"
    "v_cvt_pk_bf16_f32 v3, v10, v11\n"
    "v_mfma_f32_16x16x32_bf16 v[48:51], v[128:131], a[152:155], v[48:51]\n"
    "buffer_store_dwordx4 v[0:3], v227, s[36:39], 0 offen offset:0 sc1\n"
    "v_cvt_pk_bf16_f32 v16, v16, v17\n"
    "v_cvt_pk_bf16_f32 v17, v18, v19\n"
    "v_mfma_f32_16x16x32_bf16 v[56:59], v[128:131], a[156:159], v[56:59]\n"
    "v_cvt_pk_bf16_f32 v18, v24, v25\n"
    "v_cvt_pk_bf16_f32 v19, v26, v27\n"
    "v_mfma_f32_16x16x32_bf16 v[4:7], v[132:135], a[128:131], v[4:7]\n"
    "buffer_store_dwordx4 v[16:19], v227, s[36:39], 0 offen offset:1024 sc1\n"
    "v_cvt_pk_bf16_f32 v32, v32, v33\n"
    "v_cvt_pk_bf16_f32 v33, v34, v35\n"
    "v_mfma_f32_16x16x32_bf16 v[12:15], v[132:135], a[132:135], v[12:15]\n"
    "v_cvt_pk_bf16_f32 v34, v40, v41\n"
    "v_cvt_pk_bf16_f32 v35, v42, v43\n"
    "v_mfma_f32_16x16x32_bf16 v[20:23], v[132:135], a[136:139], v[20:23]\n"
    "buffer_store_dwordx4 v[32:35], v227, s[36:39], 0 offen offset:2048 sc1\n"
    "v_cvt_pk_bf16_f32 v48, v48, v49\n"
    "v_cvt_pk_bf16_f32 v49, v50, v51\n"
    "v_mfma_f32_16x16x32_bf16 v[28:31], v[132:135], a[140:143], v[28:31]\n"
    "v_cvt_pk_bf16_f32 v50, v56, v57\n"
    "v_cvt_pk_bf16_f32 v51, v58, v59\n"
    "v_mfma_f32_16x16x32_bf16 v[36:39], v[132:135], a[144:147], v[36:39]\n"
    "buffer_store_dwordx4 v[48:51], v227, s[36:39], 0 offen offset:3072 sc1\n"
    "v_cvt_pk_bf16_f32 v4, v4, v5\n"
    "v_cvt_pk_bf16_f32 v5, v6, v7\n"
    "v_mfma_f32_16x16x32_bf16 v[44:47], v[132:135], a[148:151], v[44:47]\n"
    "v_cvt_pk_bf16_f32 v6, v12, v13\n"
    "v_cvt_pk_bf16_f32 v7, v14, v15\n"
    "v_mfma_f32_16x16x32_bf16 v[52:55], v[132:135], a[152:155], v[52:55]\n"
    "buffer_store_dwordx4 v[4:7], v227, s[36:39], s55 offen offset:0 sc1\n"
    "v_cvt_pk_bf16_f32 v20, v20, v21\n"
    "v_cvt_pk_bf16_f32 v21, v22, v23\n"
    "v_mfma_f32_16x16x32_bf16 v[60:63], v[132:135], a[156:159], v[60:63]\n"
    "s_waitcnt lgkmcnt(1)\n"
    "v_cvt_pk_bf16_f32 v22, v28, v29\n"
    "v_cvt_pk_bf16_f32 v23, v30, v31\n"
    "v_mfma_f32_16x16x32_bf16 v[64:67], v[168:171], a[160:163], v[64:67]\n"
    "buffer_store_dwordx4 v[20:23], v227, s[36:39], s55 offen offset:1024 sc1\n"
    "ds_read_b64_tr_b16 a[128:129], v204 offset:256\n"
    "v_cvt_pk_bf16_f32 v36, v36, v37\n"
    "v_cvt_pk_bf16_f32 v37, v38, v39\n"
    "v_mfma_f32_16x16x32_bf16 v[68:71], v[172:175], a[160:163], v[68:71]\n"
    "ds_read_b64_tr_b16 a[130:131], v204 offset:8448\n"
    "v_cvt_pk_bf16_f32 v38, v44, v45\n"
    "v_cvt_pk_bf16_f32 v39, v46, v47\n"
    "v_mfma_f32_16x16x32_bf16 v[76:79], v[172:175], a[164:167], v[76:79]\n"
    "buffer_store_dwordx4 v[36:39], v227, s[36:39], s55 offen offset:2048 sc1\n"
    "ds_read_b64_tr_b16 a[132:133], v205 offset:256\n"
    "v_cvt_pk_bf16_f32 v52, v52, v53\n"
    "v_cvt_pk_bf16_f32 v53, v54, v55\n"
    "v_mfma_f32_16x16x32_bf16 v[72:75], v[168:171], a[164:167], v[72:75]\n"
    "ds_read_b64_tr_b16 a[134:135], v205 offset:8448\n"
    "v_cvt_pk_bf16_f32 v54, v60, v61\n"
    "v_cvt_pk_bf16_f32 v55, v62, v63\n"
    "v_mfma_f32_16x16x32_bf16 v[80:83], v[168:171], a[168:171], v[80:83]\n"
    "buffer_store_dwordx4 v[52:55], v227, s[36:39], s55 offen offset:3072 sc1\n"
    "ds_read_b64_tr_b16 a[136:137], v206 offset:256\n"
    "v_lshlrev_b32 v194, 16, v225\n"
    "v_mfma_f32_16x16x32_bf16 v[84:87], v[172:175], a[168:171], v[84:87]\n"
    "ds_read_b64_tr_b16 a[138:139], v206 offset:8448\n"
    "v_or_b32 v230, v255, v230\n"
    "v_mfma_f32_16x16x32_bf16 v[92:95], v[172:175], a[172:175], v[92:95]\n"
    "ds_read_b64_tr_b16 a[140:141], v207 offset:256\n"
    "v_or_b32 v248, v255, v231\n"
    "v_mfma_f32_16x16x32_bf16 v[88:91], v[168:171], a[172:175], v[88:91]\n"
    "ds_read_b64_tr_b16 a[142:143], v207 offset:8448\n"
    "v_mfma_f32_16x16x32_bf16 v[96:99], v[168:171], a[176:179], v[96:99]\n"
    "ds_read_b64_tr_b16 a[144:145], v208 offset:256\n"
    "v_readfirstlane_b32 s54, v248\n"
    "v_mfma_f32_16x16x32_bf16 v[100:103], v[172:175], a[176:179], v[100:103]\n"
    "ds_read_b64_tr_b16 a[146:147], v208 offset:8448\n"
    "v_and_b32 v222, 15, v225\n"
    "v_mfma_f32_16x16x32_bf16 v[108:111], v[172:175], a[180:183], v[108:111]\n"
    "ds_read_b64_tr_b16 a[148:149], v209 offset:256\n"
    "v_lshrrev_b32 v223, 4, v225\n"
    "v_mfma_f32_16x16x32_bf16 v[104:107], v[168:171], a[180:183], v[104:107]\n"
    "ds_read_b64_tr_b16 a[150:151], v209 offset:8448\n"
    "v_and_b32 v201, 14, v222\n"
    "v_mfma_f32_16x16x32_bf16 v[112:115], v[168:171], a[184:187], v[112:115]\n"
    "ds_read_b64_tr_b16 a[152:153], v210 offset:256\n"
    "v_xor_b32 v200, v201, v223\n"
    "v_mfma_f32_16x16x32_bf16 v[116:119], v[172:175], a[184:187], v[116:119]\n"
    "ds_read_b64_tr_b16 a[154:155], v210 offset:8448\n"
    "v_lshlrev_b32 v200, 3, v200\n"
    "v_mfma_f32_16x16x32_bf16 v[124:127], v[172:175], a[188:191], v[124:127]\n"
    "ds_read_b64_tr_b16 a[156:157], v211 offset:256\n"
    "v_lshl_add_u32 v200, v222, 7, v200\n"
    "v_mfma_f32_16x16x32_bf16 v[120:123], v[168:171], a[188:191], v[120:123]\n"
    "s_waitcnt lgkmcnt(9)\n"
    "ds_read_b64_tr_b16 a[158:159], v211 offset:8448\n"
    "v_lshl_add_u32 v200, s49, 13, v200\n"
    "v_mfma_f32_16x16x32_bf16 v[64:67], v[160:163], a[128:131], v[64:67]\n"
    "ds_read_b64_tr_b16 a[160:161], v204 offset:16640\n"
    "v_xor_b32 v201, 32, v200\n"
    "v_mfma_f32_16x16x32_bf16 v[68:71], v[164:167], a[128:131], v[68:71]\n"
    "ds_read_b64_tr_b16 a[162:163], v204 offset:24832\n"
    "v_xor_b32 v202, 64, v200\n"
    "v_mfma_f32_16x16x32_bf16 v[76:79], v[164:167], a[132:135], v[76:79]\n"
    "ds_read_b64_tr_b16 a[164:165], v205 offset:16640\n"
    "v_xor_b32 v203, 96, v200\n"
    "v_mfma_f32_16x16x32_bf16 v[72:75], v[160:163], a[132:135], v[72:75]\n"
    "ds_read_b64_tr_b16 a[166:167], v205 offset:24832\n"
    "v_xor_b32 v198, v225, v223\n"
    "v_mfma_f32_16x16x32_bf16 v[80:83], v[160:163], a[136:139], v[80:83]\n"
    "ds_read_b64_tr_b16 a[168:169], v206 offset:16640\n"
    "v_lshlrev_b32 v198, 4, v198\n"
    "v_mfma_f32_16x16x32_bf16 v[84:87], v[164:167], a[136:139], v[84:87]\n"
    "s_waitcnt lgkmcnt(5)\n"
    "ds_read_b64_tr_b16 a[170:171], v206 offset:24832\n"
    "v_lshl_add_u32 v198, s49, 13, v198\n"
    "v_mfma_f32_16x16x32_bf16 v[92:95], v[164:167], a[140:143], v[92:95]\n"
    "ds_read_b64_tr_b16 a[172:173], v207 offset:16640\n"
    "v_xor_b32 v199, 64, v198\n"
    "v_mfma_f32_16x16x32_bf16 v[88:91], v[160:163], a[140:143], v[88:91]\n"
    "ds_read_b64_tr_b16 a[174:175], v207 offset:24832\n"
    "v_lshrrev_b32 v222, 3, v225\n"
    "v_mfma_f32_16x16x32_bf16 v[96:99], v[160:163], a[144:147], v[96:99]\n"
    "ds_read_b64_tr_b16 a[176:177], v208 offset:16640\n"
    "v_and_b32 v223, 7, v225\n"
    "v_mfma_f32_16x16x32_bf16 v[100:103], v[164:167], a[144:147], v[100:103]\n"
    "ds_read_b64_tr_b16 a[178:179], v208 offset:24832\n"
    "v_lshlrev_b32 v222, 9, v222\n"
    "v_mfma_f32_16x16x32_bf16 v[108:111], v[164:167], a[148:151], v[108:111]\n"
    "ds_read_b64_tr_b16 a[180:181], v209 offset:16640\n"
    "v_lshl_add_u32 v222, v223, 4, v222\n"
    "v_mfma_f32_16x16x32_bf16 v[104:107], v[160:163], a[148:151], v[104:107]\n"
    "ds_read_b64_tr_b16 a[182:183], v209 offset:24832\n"
    "v_lshl_add_u32 v224, s52, 8, v222\n"
    "v_mfma_f32_16x16x32_bf16 v[112:115], v[160:163], a[152:155], v[112:115]\n"
    "ds_read_b64_tr_b16 a[184:185], v210 offset:16640\n"
    "s_mov_b32 s96, 0x00300003\n"
    "v_and_b32 v196, 1, v225\n"
    "v_mfma_f32_16x16x32_bf16 v[116:119], v[164:167], a[152:155], v[116:119]\n"
    "ds_read_b64_tr_b16 a[186:187], v210 offset:24832\n"
    "s_mov_b32 s97, 0x30000300\n"
    "v_sub_u32 v196, 0, v196\n"
    "v_mfma_f32_16x16x32_bf16 v[124:127], v[164:167], a[156:159], v[124:127]\n"
    "ds_read_b64_tr_b16 a[188:189], v211 offset:16640\n"
    "v_xor_b32 v196, 0xffff, v196\n"
    "v_mfma_f32_16x16x32_bf16 v[120:123], v[160:163], a[156:159], v[120:123]\n"
    "s_waitcnt lgkmcnt(9)\n"
    "ds_read_b64_tr_b16 a[190:191], v211 offset:24832\n"
    "v_cndmask_b32_e64 v195, 0, v196, s[96:97]\n"
    "v_mfma_f32_16x16x32_bf16 v[64:67], v[184:187], a[160:163], v[64:67]\n"
    "ds_read_b64_tr_b16 a[128:129], v204 offset:33024\n"
    "s_lshl_b64 s[96:97], s[96:97], 2\n"
    "v_mfma_f32_16x16x32_bf16 v[68:71], v[188:191], a[160:163], v[68:71]\n"
    "ds_read_b64_tr_b16 a[130:131], v204 offset:41216\n"
    "v_cndmask_b32_e64 v196, 0, v196, s[96:97]\n"
    "v_mfma_f32_16x16x32_bf16 v[76:79], v[188:191], a[164:167], v[76:79]\n"
    "ds_read_b64_tr_b16 a[132:133], v205 offset:33024\n"
    "v_mfma_f32_16x16x32_bf16 v[72:75], v[184:187], a[164:167], v[72:75]\n"
    "ds_read_b64_tr_b16 a[134:135], v205 offset:41216\n"
    "v_mfma_f32_16x16x32_bf16 v[80:83], v[184:187], a[168:171], v[80:83]\n"
    "ds_read_b64_tr_b16 a[136:137], v206 offset:33024\n"
    "v_mfma_f32_16x16x32_bf16 v[84:87], v[188:191], a[168:171], v[84:87]\n"
    "s_waitcnt lgkmcnt(5)\n"
    "ds_read_b64_tr_b16 a[138:139], v206 offset:41216\n"
    "v_mfma_f32_16x16x32_bf16 v[92:95], v[188:191], a[172:175], v[92:95]\n"
    "ds_read_b64_tr_b16 a[140:141], v207 offset:33024\n"
    "v_mfma_f32_16x16x32_bf16 v[88:91], v[184:187], a[172:175], v[88:91]\n"
    "ds_read_b64_tr_b16 a[142:143], v207 offset:41216\n"
    "v_mfma_f32_16x16x32_bf16 v[96:99], v[184:187], a[176:179], v[96:99]\n"
    "ds_read_b64_tr_b16 a[144:145], v208 offset:33024\n"
    "v_mfma_f32_16x16x32_bf16 v[100:103], v[188:191], a[176:179], v[100:103]\n"
    "ds_read_b64_tr_b16 a[146:147], v208 offset:41216\n"
    "v_mfma_f32_16x16x32_bf16 v[108:111], v[188:191], a[180:183], v[108:111]\n"
    "ds_read_b64_tr_b16 a[148:149], v209 offset:33024\n"
    "v_mfma_f32_16x16x32_bf16 v[104:107], v[184:187], a[180:183], v[104:107]\n"
    "ds_read_b64_tr_b16 a[150:151], v209 offset:41216\n"
    "v_mfma_f32_16x16x32_bf16 v[112:115], v[184:187], a[184:187], v[112:115]\n"
    "ds_read_b64_tr_b16 a[152:153], v210 offset:33024\n"
    "v_mfma_f32_16x16x32_bf16 v[116:119], v[188:191], a[184:187], v[116:119]\n"
    "ds_read_b64_tr_b16 a[154:155], v210 offset:41216\n"
    "v_mfma_f32_16x16x32_bf16 v[124:127], v[188:191], a[188:191], v[124:127]\n"
    "ds_read_b64_tr_b16 a[156:157], v211 offset:33024\n"
    "v_mfma_f32_16x16x32_bf16 v[120:123], v[184:187], a[188:191], v[120:123]\n"
    "s_waitcnt lgkmcnt(9)\n"
    "ds_read_b64_tr_b16 a[158:159], v211 offset:41216\n"
    "v_mfma_f32_16x16x32_bf16 v[64:67], v[212:215], a[128:131], v[64:67]\n"
    "ds_read_b64_tr_b16 a[160:161], v204 offset:49408\n"
    "v_mfma_f32_16x16x32_bf16 v[68:71], v[216:219], a[128:131], v[68:71]\n"
    "ds_read_b64_tr_b16 a[162:163], v204 offset:57600\n"
    "v_subrev_u32 v204, 32768, v204\n"
    "v_mfma_f32_16x16x32_bf16 v[76:79], v[216:219], a[132:135], v[76:79]\n"
    "ds_read_b64_tr_b16 a[164:165], v205 offset:49408\n"
    "v_mfma_f32_16x16x32_bf16 v[72:75], v[212:215], a[132:135], v[72:75]\n"
    "ds_read_b64_tr_b16 a[166:167], v205 offset:57600\n"
    "v_subrev_u32 v205, 32768, v205\n"
    "v_mfma_f32_16x16x32_bf16 v[80:83], v[212:215], a[136:139], v[80:83]\n"
    "ds_read_b64_tr_b16 a[168:169], v206 offset:49408\n"
    "v_mfma_f32_16x16x32_bf16 v[84:87], v[216:219], a[136:139], v[84:87]\n"
    "s_waitcnt lgkmcnt(5)\n"
    "ds_read_b64_tr_b16 a[170:171], v206 offset:57600\n"
    "v_subrev_u32 v206, 32768, v206\n"
    "v_mfma_f32_16x16x32_bf16 v[92:95], v[216:219], a[140:143], v[92:95]\n"
    "ds_read_b64_tr_b16 a[172:173], v207 offset:49408\n"
    "v_mfma_f32_16x16x32_bf16 v[88:91], v[212:215], a[140:143], v[88:91]\n"
    "ds_read_b64_tr_b16 a[174:175], v207 offset:57600\n"
    "v_subrev_u32 v207, 32768, v207\n"
    "v_mfma_f32_16x16x32_bf16 v[96:99], v[212:215], a[144:147], v[96:99]\n"
    "ds_read_b64_tr_b16 a[176:177], v208 offset:49408\n"
    "v_mfma_f32_16x16x32_bf16 v[100:103], v[216:219], a[144:147], v[100:103]\n"
    "ds_read_b64_tr_b16 a[178:179], v208 offset:57600\n"
    "v_subrev_u32 v208, 32768, v208\n"
    "v_mfma_f32_16x16x32_bf16 v[108:111], v[216:219], a[148:151], v[108:111]\n"
    "ds_read_b64_tr_b16 a[180:181], v209 offset:49408\n"
    "v_mfma_f32_16x16x32_bf16 v[104:107], v[212:215], a[148:151], v[104:107]\n"
    "ds_read_b64_tr_b16 a[182:183], v209 offset:57600\n"
    "v_subrev_u32 v209, 32768, v209\n"
    "v_mfma_f32_16x16x32_bf16 v[112:115], v[212:215], a[152:155], v[112:115]\n"
    "ds_read_b64_tr_b16 a[184:185], v210 offset:49408\n"
    "v_mfma_f32_16x16x32_bf16 v[116:119], v[216:219], a[152:155], v[116:119]\n"
    "ds_read_b64_tr_b16 a[186:187], v210 offset:57600\n"
    "v_subrev_u32 v210, 32768, v210\n"
    "v_mfma_f32_16x16x32_bf16 v[124:127], v[216:219], a[156:159], v[124:127]\n"
    "ds_read_b64_tr_b16 a[188:189], v211 offset:49408\n"
    "v_mfma_f32_16x16x32_bf16 v[120:123], v[212:215], a[156:159], v[120:123]\n"
    "s_waitcnt lgkmcnt(9)\n"
    "ds_read_b64_tr_b16 a[190:191], v211 offset:57600\n"
    "v_subrev_u32 v211, 32768, v211\n"
    "v_mfma_f32_16x16x32_bf16 v[64:67], v[128:131], a[160:163], v[64:67]\n"
    "ds_read_b64_tr_b16 a[128:129], v204 offset:256\n"
    "v_mfma_f32_16x16x32_bf16 v[68:71], v[132:135], a[160:163], v[68:71]\n"
    "ds_read_b64_tr_b16 a[130:131], v204 offset:8448\n"
    "v_mfma_f32_16x16x32_bf16 v[76:79], v[132:135], a[164:167], v[76:79]\n"
    "ds_read_b64_tr_b16 a[132:133], v205 offset:256\n"
    "v_mfma_f32_16x16x32_bf16 v[72:75], v[128:131], a[164:167], v[72:75]\n"
    "ds_read_b64_tr_b16 a[134:135], v205 offset:8448\n"
    "v_mfma_f32_16x16x32_bf16 v[80:83], v[128:131], a[168:171], v[80:83]\n"
    "ds_read_b64_tr_b16 a[136:137], v206 offset:256\n"
    "v_mfma_f32_16x16x32_bf16 v[84:87], v[132:135], a[168:171], v[84:87]\n"
    "s_waitcnt lgkmcnt(5)\n"
    "ds_read_b64_tr_b16 a[138:139], v206 offset:8448\n"
    "v_mfma_f32_16x16x32_bf16 v[92:95], v[132:135], a[172:175], v[92:95]\n"
    "ds_read_b64_tr_b16 a[140:141], v207 offset:256\n"
    "v_mfma_f32_16x16x32_bf16 v[88:91], v[128:131], a[172:175], v[88:91]\n"
    "ds_read_b64_tr_b16 a[142:143], v207 offset:8448\n"
    "v_mfma_f32_16x16x32_bf16 v[96:99], v[128:131], a[176:179], v[96:99]\n"
    "ds_read_b64_tr_b16 a[144:145], v208 offset:256\n"
    "v_mfma_f32_16x16x32_bf16 v[100:103], v[132:135], a[176:179], v[100:103]\n"
    "ds_read_b64_tr_b16 a[146:147], v208 offset:8448\n"
    "v_mfma_f32_16x16x32_bf16 v[108:111], v[132:135], a[180:183], v[108:111]\n"
    "ds_read_b64_tr_b16 a[148:149], v209 offset:256\n"
    "v_mfma_f32_16x16x32_bf16 v[104:107], v[128:131], a[180:183], v[104:107]\n"
    "ds_read_b64_tr_b16 a[150:151], v209 offset:8448\n"
    "v_mfma_f32_16x16x32_bf16 v[112:115], v[128:131], a[184:187], v[112:115]\n"
    "ds_read_b64_tr_b16 a[152:153], v210 offset:256\n"
    "v_mfma_f32_16x16x32_bf16 v[116:119], v[132:135], a[184:187], v[116:119]\n"
    "ds_read_b64_tr_b16 a[154:155], v210 offset:8448\n"
    "v_mfma_f32_16x16x32_bf16 v[124:127], v[132:135], a[188:191], v[124:127]\n"
    "ds_read_b64_tr_b16 a[156:157], v211 offset:256\n"
    "v_mfma_f32_16x16x32_bf16 v[120:123], v[128:131], a[188:191], v[120:123]\n"
    "s_waitcnt lgkmcnt(9)\n"
    "s_waitcnt vmcnt(0)\n"
    "buffer_store_dword v230, v194, s[36:39], s58 offen offset:8 sc1\n"
    "ds_read_b64_tr_b16 a[158:159], v211 offset:8448\n"
    "v_mfma_f32_16x16x32_bf16 v[64:67], v[232:235], a[128:131], v[64:67]\n"
    "ds_read_b64_tr_b16 a[160:161], v204 offset:16640\n"
    "v_mfma_f32_16x16x32_bf16 v[68:71], v[236:239], a[128:131], v[68:71]\n"
    "ds_read_b64_tr_b16 a[162:163], v204 offset:24832\n"
    "v_mfma_f32_16x16x32_bf16 v[76:79], v[236:239], a[132:135], v[76:79]\n"
    "ds_read_b64_tr_b16 a[164:165], v205 offset:16640\n"
    "v_mfma_f32_16x16x32_bf16 v[72:75], v[232:235], a[132:135], v[72:75]\n"
    "ds_read_b64_tr_b16 a[166:167], v205 offset:24832\n"
    "v_mfma_f32_16x16x32_bf16 v[80:83], v[232:235], a[136:139], v[80:83]\n"
    "ds_read_b64_tr_b16 a[168:169], v206 offset:16640\n"
    "v_mfma_f32_16x16x32_bf16 v[84:87], v[236:239], a[136:139], v[84:87]\n"
    "s_waitcnt lgkmcnt(5)\n"
    "ds_read_b64_tr_b16 a[170:171], v206 offset:24832\n"
    "v_mfma_f32_16x16x32_bf16 v[92:95], v[236:239], a[140:143], v[92:95]\n"
    "ds_read_b64_tr_b16 a[172:173], v207 offset:16640\n"
    "v_mfma_f32_16x16x32_bf16 v[88:91], v[232:235], a[140:143], v[88:91]\n"
    "ds_read_b64_tr_b16 a[174:175], v207 offset:24832\n"
    "v_mfma_f32_16x16x32_bf16 v[96:99], v[232:235], a[144:147], v[96:99]\n"
    "ds_read_b64_tr_b16 a[176:177], v208 offset:16640\n"
    "v_mfma_f32_16x16x32_bf16 v[100:103], v[236:239], a[144:147], v[100:103]\n"
    "ds_read_b64_tr_b16 a[178:179], v208 offset:24832\n"
    "v_mfma_f32_16x16x32_bf16 v[108:111], v[236:239], a[148:151], v[108:111]\n"
    "ds_read_b64_tr_b16 a[180:181], v209 offset:16640\n"
    "v_mfma_f32_16x16x32_bf16 v[104:107], v[232:235], a[148:151], v[104:107]\n"
    "ds_read_b64_tr_b16 a[182:183], v209 offset:24832\n"
    "v_mfma_f32_16x16x32_bf16 v[112:115], v[232:235], a[152:155], v[112:115]\n"
    "ds_read_b64_tr_b16 a[184:185], v210 offset:16640\n"
    "v_mfma_f32_16x16x32_bf16 v[116:119], v[236:239], a[152:155], v[116:119]\n"
    "ds_read_b64_tr_b16 a[186:187], v210 offset:24832\n"
    "v_mfma_f32_16x16x32_bf16 v[124:127], v[236:239], a[156:159], v[124:127]\n"
    "ds_read_b64_tr_b16 a[188:189], v211 offset:16640\n"
    "v_mfma_f32_16x16x32_bf16 v[120:123], v[232:235], a[156:159], v[120:123]\n"
    "s_waitcnt lgkmcnt(9)\n"
    "ds_read_b64_tr_b16 a[190:191], v211 offset:24832\n"
    "v_mfma_f32_16x16x32_bf16 v[64:67], v[240:243], a[160:163], v[64:67]\n"
    "v_mfma_f32_16x16x32_bf16 v[68:71], v[244:247], a[160:163], v[68:71]\n"
    "v_mfma_f32_16x16x32_bf16 v[76:79], v[244:247], a[164:167], v[76:79]\n"
    "v_mfma_f32_16x16x32_bf16 v[72:75], v[240:243], a[164:167], v[72:75]\n"
    "v_mfma_f32_16x16x32_bf16 v[80:83], v[240:243], a[168:171], v[80:83]\n"
    "v_cvt_pk_bf16_f32 v0, v64, v65\n"
    "v_cvt_pk_bf16_f32 v1, v66, v67\n"
    "v_mfma_f32_16x16x32_bf16 v[84:87], v[244:247], a[168:171], v[84:87]\n"
    "s_waitcnt lgkmcnt(0)\n"
    "v_cvt_pk_bf16_f32 v32, v68, v69\n"
    "v_cvt_pk_bf16_f32 v33, v70, v71\n"
    "v_mfma_f32_16x16x32_bf16 v[92:95], v[244:247], a[172:175], v[92:95]\n"
    "v_cvt_pk_bf16_f32 v38, v76, v77\n"
    "v_cvt_pk_bf16_f32 v39, v78, v79\n"
    "v_mfma_f32_16x16x32_bf16 v[88:91], v[240:243], a[172:175], v[88:91]\n"
    "v_cvt_pk_bf16_f32 v6, v72, v73\n"
    "v_cvt_pk_bf16_f32 v7, v74, v75\n"
    "v_mfma_f32_16x16x32_bf16 v[96:99], v[240:243], a[176:179], v[96:99]\n"
    "v_cvt_pk_bf16_f32 v8, v80, v81\n"
    "v_cvt_pk_bf16_f32 v9, v82, v83\n"
    "v_mfma_f32_16x16x32_bf16 v[100:103], v[244:247], a[176:179], v[100:103]\n"
    "v_cvt_pk_bf16_f32 v40, v84, v85\n"
    "v_cvt_pk_bf16_f32 v41, v86, v87\n"
    "v_mfma_f32_16x16x32_bf16 v[108:111], v[244:247], a[180:183], v[108:111]\n"
    "v_cvt_pk_bf16_f32 v46, v92, v93\n"
    "v_cvt_pk_bf16_f32 v47, v94, v95\n"
    "v_mfma_f32_16x16x32_bf16 v[104:107], v[240:243], a[180:183], v[104:107]\n"
    "v_cvt_pk_bf16_f32 v14, v88, v89\n"
    "v_cvt_pk_bf16_f32 v15, v90, v91\n"
    "v_mfma_f32_16x16x32_bf16 v[112:115], v[240:243], a[184:187], v[112:115]\n"
    "v_cvt_pk_bf16_f32 v16, v96, v97\n"
    "v_cvt_pk_bf16_f32 v17, v98, v99\n"
    "v_mfma_f32_16x16x32_bf16 v[116:119], v[244:247], a[184:187], v[116:119]\n"
    "buffer_load_dwordx4 v[176:179], v226, s[40:43], s58 offen sc1\n"
    "v_cvt_pk_bf16_f32 v48, v100, v101\n"
    "v_cvt_pk_bf16_f32 v49, v102, v103\n"
    "v_mfma_f32_16x16x32_bf16 v[124:127], v[244:247], a[188:191], v[124:127]\n"
    "v_cvt_pk_bf16_f32 v54, v108, v109\n"
    "v_cvt_pk_bf16_f32 v55, v110, v111\n"
    "v_mfma_f32_16x16x32_bf16 v[120:123], v[240:243], a[188:191], v[120:123]\n"
    "v_cvt_pk_bf16_f32 v22, v104, v105\n"
    "v_cvt_pk_bf16_f32 v23, v106, v107\n"
    "v_cvt_pk_bf16_f32 v24, v112, v113\n"
    "v_cvt_pk_bf16_f32 v25, v114, v115\n"
    "v_cvt_pk_bf16_f32 v56, v116, v117\n"
    "v_cvt_pk_bf16_f32 v57, v118, v119\n"
    "v_cvt_pk_bf16_f32 v62, v124, v125\n"
    "v_cvt_pk_bf16_f32 v63, v126, v127\n"
    "v_cvt_pk_bf16_f32 v30, v120, v121\n"
    "v_cvt_pk_bf16_f32 v31, v122, v123\n"
    "s_mov_b32 s56, 12288\n"
    "s_mov_b32 s48, 0x40000\n"
    "s_waitcnt vmcnt(0)\n"
    "v_cmp_ne_u64 vcc, v[178:179], v[248:249]\n"
    "v_or_b32 v178, v255, v178\n"
    "s_cbranch_vccz .Lgo_%=\n"
    "v_cmp_ne_u64 vcc, v[178:179], v[248:249]\n"
    "s_cbranch_vccnz .Lspin_%=\n"
    ".Lfast_%=:\n"
    "buffer_load_dword v180, v194, s[40:43], s58 offen offset:8 sc1\n"
    "s_waitcnt vmcnt(0)\n"
    "v_readfirstlane_b32 s53, v180\n"
    "s_cmp_eq_u32 s53, s54\n"
    "s_cbranch_scc1 .Lgo_%=\n"
    "s_sub_u32 s48, s48, 1\n"
    "s_cbranch_scc0 .Lfast_%=\n"
    ".Lgo_%=:\n"
    "buffer_load_dwordx4 v[2:5], v227, s[40:43], 0 offen offset:0 sc1\n"
    "buffer_load_dwordx4 v[10:13], v227, s[40:43], 0 offen offset:1024 sc1\n"
    "buffer_load_dwordx4 v[18:21], v227, s[40:43], 0 offen offset:2048 sc1\n"
    "buffer_load_dwordx4 v[26:29], v227, s[40:43], 0 offen offset:3072 sc1\n"
    "buffer_load_dwordx4 v[34:37], v227, s[40:43], s55 offen offset:0 sc1\n"
    "buffer_load_dwordx4 v[42:45], v227, s[40:43], s55 offen offset:1024 sc1\n"
    "buffer_load_dwordx4 v[50:53], v227, s[40:43], s55 offen offset:2048 sc1\n"
    "buffer_load_dwordx4 v[58:61], v227, s[40:43], s55 offen offset:3072 sc1\n"
    "v_min_f32 v178, v193, v177\n"
    "v_sub_f32 v248, v178, v193\n"
    "v_sub_f32 v250, v178, v177\n"
    "v_exp_f32 v248, v248\n"
    "v_exp_f32 v250, v250\n"
    "v_mul_f32 v178, v248, v228\n"
    "v_fma_f32 v178, v250, v176, v178\n"
    "v_rcp_f32 v178, v178\n"
    "s_nop 0\n"
    "v_mul_f32 v248, v248, v178\n"
    "v_mul_f32 v250, v250, v178\n"
    "v_mov_b32 v249, v248\n"
    "v_mov_b32 v251, v250\n"
    "s_nop 0\n"
    "v_permlane32_swap_b32 v248, v249\n"
    "v_permlane32_swap_b32 v250, v251\n"
    "v_cvt_pk_bf16_f32 v161, v248, v248\n"
    "v_and_b32 v180, 0xffff0000, v161\n"
    "v_sub_f32 v180, v248, v180\n"
    "v_cvt_pk_bf16_f32 v167, v180, v180\n"
    "v_and_b32 v160, v195, v161\n"
    "v_and_b32 v161, v196, v161\n"
    "v_pk_mov_b32 v[164:165], v[160:161], v[160:161] op_sel:[0,1]\n"
    "v_and_b32 v166, v195, v167\n"
    "v_and_b32 v167, v196, v167\n"
    "v_pk_mov_b32 v[170:171], v[166:167], v[166:167] op_sel:[0,1]\n"
    "v_cvt_pk_bf16_f32 v163, v250, v250\n"
    "v_and_b32 v181, 0xffff0000, v163\n"
    "v_sub_f32 v181, v250, v181\n"
    "v_cvt_pk_bf16_f32 v169, v181, v181\n"
    "v_and_b32 v162, v195, v163\n"
    "v_and_b32 v163, v196, v163\n"
    "v_and_b32 v168, v195, v169\n"
    "v_and_b32 v169, v196, v169\n"
    "v_cvt_pk_bf16_f32 v185, v249, v249\n"
    "v_and_b32 v180, 0xffff0000, v185\n"
    "v_sub_f32 v180, v249, v180\n"
    "v_cvt_pk_bf16_f32 v233, v180, v180\n"
    "v_and_b32 v184, v195, v185\n"
    "v_and_b32 v185, v196, v185\n"
    "v_pk_mov_b32 v[188:189], v[184:185], v[184:185] op_sel:[0,1]\n"
    "v_and_b32 v232, v195, v233\n"
    "v_and_b32 v233, v196, v233\n"
    "v_pk_mov_b32 v[236:237], v[232:233], v[232:233] op_sel:[0,1]\n"
    "v_cvt_pk_bf16_f32 v187, v251, v251\n"
    "v_and_b32 v181, 0xffff0000, v187\n"
    "v_sub_f32 v181, v251, v181\n"
    "v_cvt_pk_bf16_f32 v235, v181, v181\n"
    "v_and_b32 v186, v195, v187\n"
    "v_and_b32 v187, v196, v187\n"
    "v_and_b32 v234, v195, v235\n"
    "v_and_b32 v235, v196, v235\n"
    "s_waitcnt vmcnt(7)\n"
    "v_mfma_f32_16x16x32_bf16 v[64:67], v[0:3], v[160:163], 0\n"
    "v_mfma_f32_16x16x32_bf16 v[64:67], v[0:3], v[166:169], v[64:67]\n"
    "v_mfma_f32_16x16x32_bf16 v[68:71], v[4:7], v[162:165], 0\n"
    "v_mfma_f32_16x16x32_bf16 v[68:71], v[4:7], v[168:171], v[68:71]\n"
    "s_waitcnt vmcnt(6)\n"
    "v_mfma_f32_16x16x32_bf16 v[72:75], v[8:11], v[160:163], 0\n"
    "s_nop 1\n"
    "v_cvt_pk_bf16_f32 v64, v64, v65\n"
    "v_cvt_pk_bf16_f32 v65, v66, v67\n"
    "v_mfma_f32_16x16x32_bf16 v[72:75], v[8:11], v[166:169], v[72:75]\n"
    "ds_write_b64 v200, v[64:65] offset:0\n"
    "v_mfma_f32_16x16x32_bf16 v[76:79], v[12:15], v[162:165], 0\n"
    "v_cvt_pk_bf16_f32 v68, v68, v69\n"
    "v_cvt_pk_bf16_f32 v69, v70, v71\n"
    "v_mfma_f32_16x16x32_bf16 v[76:79], v[12:15], v[168:171], v[76:79]\n"
    "ds_write_b64 v201, v[68:69] offset:0\n"
    "s_waitcnt vmcnt(5)\n"
    "v_mfma_f32_16x16x32_bf16 v[80:83], v[16:19], v[160:163], 0\n"
    "v_cvt_pk_bf16_f32 v72, v72, v73\n"
    "v_cvt_pk_bf16_f32 v73, v74, v75\n"
    "v_mfma_f32_16x16x32_bf16 v[80:83], v[16:19], v[166:169], v[80:83]\n"
    "ds_write_b64 v202, v[72:73] offset:0\n"
    "v_mfma_f32_16x16x32_bf16 v[84:87], v[20:23], v[162:165], 0\n"
    "v_cvt_pk_bf16_f32 v76, v76, v77\n"
    "v_cvt_pk_bf16_f32 v77, v78, v79\n"
    "v_mfma_f32_16x16x32_bf16 v[84:87], v[20:23], v[168:171], v[84:87]\n"
    "ds_write_b64 v203, v[76:77] offset:0\n"
    "ds_read_b128 v[128:131], v198 offset:0\n"
    "ds_read_b128 v[132:135], v199 offset:1024\n"
    "s_waitcnt vmcnt(4)\n"
    "v_mfma_f32_16x16x32_bf16 v[88:91], v[24:27], v[160:163], 0\n"
    "v_cvt_pk_bf16_f32 v80, v80, v81\n"
    "v_cvt_pk_bf16_f32 v81, v82, v83\n"
    "v_mfma_f32_16x16x32_bf16 v[88:91], v[24:27], v[166:169], v[88:91]\n"
    "ds_write_b64 v200, v[80:81] offset:2048\n"
    "v_mfma_f32_16x16x32_bf16 v[92:95], v[28:31], v[162:165], 0\n"
    "v_cvt_pk_bf16_f32 v84, v84, v85\n"
    "v_cvt_pk_bf16_f32 v85, v86, v87\n"
    "v_mfma_f32_16x16x32_bf16 v[92:95], v[28:31], v[168:171], v[92:95]\n"
    "s_waitcnt lgkmcnt(2)\n"
    "buffer_store_dwordx4 v[128:131], v224, s[16:19], 0 offen offset:0 sc1\n"
    "ds_write_b64 v201, v[84:85] offset:2048\n"
    "s_waitcnt vmcnt(4)\n"
    "v_mfma_f32_16x16x32_bf16 v[96:99], v[32:35], v[184:187], 0\n"
    "v_cvt_pk_bf16_f32 v88, v88, v89\n"
    "v_cvt_pk_bf16_f32 v89, v90, v91\n"
    "v_mfma_f32_16x16x32_bf16 v[96:99], v[32:35], v[232:235], v[96:99]\n"
    "s_waitcnt lgkmcnt(2)\n"
    "buffer_store_dwordx4 v[132:135], v224, s[16:19], s55 offen offset:0 sc1\n"
    "ds_write_b64 v202, v[88:89] offset:2048\n"
    "v_mfma_f32_16x16x32_bf16 v[100:103], v[36:39], v[186:189], 0\n"
    "v_cvt_pk_bf16_f32 v92, v92, v93\n"
    "v_cvt_pk_bf16_f32 v93, v94, v95\n"
    "v_mfma_f32_16x16x32_bf16 v[100:103], v[36:39], v[234:237], v[100:103]\n"
    "ds_write_b64 v203, v[92:93] offset:2048\n"
    "ds_read_b128 v[136:139], v198 offset:2048\n"
    "ds_read_b128 v[140:143], v199 offset:3072\n"
    "s_waitcnt vmcnt(4)\n"
    "v_mfma_f32_16x16x32_bf16 v[104:107], v[40:43], v[184:187], 0\n"
    "v_cvt_pk_bf16_f32 v96, v96, v97\n"
    "v_cvt_pk_bf16_f32 v97, v98, v99\n"
    "v_mfma_f32_16x16x32_bf16 v[104:107], v[40:43], v[232:235], v[104:107]\n"
    "ds_write_b64 v200, v[96:97] offset:4096\n"
    "v_mfma_f32_16x16x32_bf16 v[108:111], v[44:47], v[186:189], 0\n"
    "v_cvt_pk_bf16_f32 v100, v100, v101\n"
    "v_cvt_pk_bf16_f32 v101, v102, v103\n"
    "v_mfma_f32_16x16x32_bf16 v[108:111], v[44:47], v[234:237], v[108:111]\n"
    "s_waitcnt lgkmcnt(2)\n"
    "buffer_store_dwordx4 v[136:139], v224, s[16:19], 0 offen offset:128 sc1\n"
    "ds_write_b64 v201, v[100:101] offset:4096\n"
    "s_waitcnt vmcnt(4)\n"
    "v_mfma_f32_16x16x32_bf16 v[112:115], v[48:51], v[184:187], 0\n"
    "v_cvt_pk_bf16_f32 v104, v104, v105\n"
    "v_cvt_pk_bf16_f32 v105, v106, v107\n"
    "v_mfma_f32_16x16x32_bf16 v[112:115], v[48:51], v[232:235], v[112:115]\n"
    "s_waitcnt lgkmcnt(2)\n"
    "buffer_store_dwordx4 v[140:143], v224, s[16:19], s55 offen offset:128 sc1\n"
    "ds_write_b64 v202, v[104:105] offset:4096\n"
    "v_mfma_f32_16x16x32_bf16 v[116:119], v[52:55], v[186:189], 0\n"
    "v_cvt_pk_bf16_f32 v108, v108, v109\n"
    "v_cvt_pk_bf16_f32 v109, v110, v111\n"
    "v_mfma_f32_16x16x32_bf16 v[116:119], v[52:55], v[234:237], v[116:119]\n"
    "ds_write_b64 v203, v[108:109] offset:4096\n"
    "ds_read_b128 v[144:147], v198 offset:4096\n"
    "ds_read_b128 v[148:151], v199 offset:5120\n"
    "s_waitcnt vmcnt(4)\n"
    "v_mfma_f32_16x16x32_bf16 v[120:123], v[56:59], v[184:187], 0\n"
    "v_cvt_pk_bf16_f32 v112, v112, v113\n"
    "v_cvt_pk_bf16_f32 v113, v114, v115\n"
    "v_mfma_f32_16x16x32_bf16 v[120:123], v[56:59], v[232:235], v[120:123]\n"
    "ds_write_b64 v200, v[112:113] offset:6144\n"
    "v_mfma_f32_16x16x32_bf16 v[124:127], v[60:63], v[186:189], 0\n"
    "v_cvt_pk_bf16_f32 v116, v116, v117\n"
    "v_cvt_pk_bf16_f32 v117, v118, v119\n"
    "v_mfma_f32_16x16x32_bf16 v[124:127], v[60:63], v[234:237], v[124:127]\n"
    "s_waitcnt lgkmcnt(2)\n"
    "buffer_store_dwordx4 v[144:147], v224, s[16:19], s58 offen offset:0 sc1\n"
    "ds_write_b64 v201, v[116:117] offset:6144\n"
    "s_nop 1\n"
    "v_cvt_pk_bf16_f32 v120, v120, v121\n"
    "v_cvt_pk_bf16_f32 v121, v122, v123\n"
    "s_waitcnt lgkmcnt(2)\n"
    "buffer_store_dwordx4 v[148:151], v224, s[16:19], s56 offen offset:0 sc1\n"
    "ds_write_b64 v202, v[120:121] offset:6144\n"
    "s_nop 2\n"
    "v_cvt_pk_bf16_f32 v124, v124, v125\n"
    "v_cvt_pk_bf16_f32 v125, v126, v127\n"
    "ds_write_b64 v203, v[124:125] offset:6144\n"
    "ds_read_b128 v[152:155], v198 offset:6144\n"
    "ds_read_b128 v[156:159], v199 offset:7168\n"
    "s_waitcnt lgkmcnt(1)\n"
    "buffer_store_dwordx4 v[152:155], v224, s[16:19], s58 offen offset:128 sc1\n"
    "s_waitcnt lgkmcnt(0)\n"
    "buffer_store_dwordx4 v[156:159], v224, s[16:19], s56 offen offset:128 sc1\n"
    "s_endpgm\n"
    ".Lspin_%=:\n"
    "v_readfirstlane_b32 s53, v179\n"
    "s_nop 1\n"
    "v_cmp_ne_u32 vcc, s53, v249\n"
    "s_cbranch_vccz .Lagain_%=\n"
    "v_mov_b32 v249, s53\n"
    "v_or_b32 v230, s53, v255\n"
    "s_waitcnt vmcnt(0)\n"
    "buffer_store_dwordx4 v[228:231], v252, s[36:39], s58 offen sc1\n"
    ".Lagain_%=:\n"
    "buffer_load_dwordx4 v[176:179], v226, s[40:43], s58 offen sc1\n"
    "s_sub_u32 s48, s48, 1\n"
    "s_waitcnt vmcnt(0)\n"
    "s_cbranch_scc1 .Lgo_%=\n"
    "v_or_b32 v178, v255, v178\n"
    "v_cmp_ne_u64 vcc, v[178:179], v[248:249]\n"
    "s_cbranch_vccnz .Lspin_%=\n"
    "s_branch .Lfast_%=\n"
    ".Lstub_b0_%=:\n"
    "s_mov_b32 s59, 0\n"
    "s_call_b64 s[50:51], .Lslow_0_%=\n"
    "s_cbranch_scc1 .Lret_b0_%=\n"
    "v_mov_b32 v160, v128\n"
    "v_mov_b32 v161, v130\n"
    "v_mov_b32 v162, v132\n"
    "v_mov_b32 v163, v134\n"
    "v_mov_b32 v164, v136\n"
    "v_mov_b32 v165, v138\n"
    "v_mov_b32 v166, v140\n"
    "v_mov_b32 v167, v142\n"
    "s_nop 1\n"
    "s_branch .Lret_b0_%=\n"
    ".Lstub_p1_%=:\n"
    "s_mov_b32 s59, 16384\n"
    "s_call_b64 s[50:51], .Lslow_1_%=\n"
    "s_cbranch_scc1 .Lret_p1_%=\n"
    "v_mov_b32 v168, v144\n"
    "v_mov_b32 v169, v146\n"
    "v_mov_b32 v170, v148\n"
    "v_mov_b32 v171, v150\n"
    "v_mov_b32 v172, v152\n"
    "v_mov_b32 v173, v154\n"
    "v_mov_b32 v174, v156\n"
    "v_mov_b32 v175, v158\n"
    "s_nop 1\n"
    "s_branch .Lret_p1_%=\n"
    ".Lstub_p2_%=:\n"
    "s_mov_b32 s59, 32768\n"
    "s_call_b64 s[50:51], .Lslow_0_%=\n"
    "s_cbranch_scc1 .Lret_p2_%=\n"
    "v_mov_b32 v160, v128\n"
    "v_mov_b32 v161, v130\n"
    "v_mov_b32 v162, v132\n"
    "v_mov_b32 v163, v134\n"
    "v_mov_b32 v164, v136\n"
    "v_mov_b32 v165, v138\n"
    "v_mov_b32 v166, v140\n"
    "v_mov_b32 v167, v142\n"
    "s_nop 1\n"
    "s_branch .Lret_p2_%=\n"
    ".Lstub_p3_%=:\n"
    "s_mov_b32 s59, 49152\n"
    "s_call_b64 s[50:51], .Lslow_1_%=\n"
    "s_cbranch_scc1 .Lret_p3_%=\n"
    "v_mov_b32 v168, v144\n"
    "v_mov_b32 v169, v146\n"
    "v_mov_b32 v170, v148\n"
    "v_mov_b32 v171, v150\n"
    "v_mov_b32 v172, v152\n"
    "v_mov_b32 v173, v154\n"
    "v_mov_b32 v174, v156\n"
    "v_mov_b32 v175, v158\n"
    "s_nop 1\n"
    "s_branch .Lret_p3_%=\n"
    ".Lstub_p0_%=:\n"
    "s_mov_b32 s59, 0\n"
    "s_call_b64 s[50:51], .Lslow_0_%=\n"
    "s_cbranch_scc1 .Lret_p0_%=\n"
    "v_mov_b32 v160, v128\n"
    "v_mov_b32 v161, v130\n"
    "v_mov_b32 v162, v132\n"
    "v_mov_b32 v163, v134\n"
    "v_mov_b32 v164, v136\n"
    "v_mov_b32 v165, v138\n"
    "v_mov_b32 v166, v140\n"
    "v_mov_b32 v167, v142\n"
    "s_nop 1\n"
    "s_branch .Lret_p0_%=\n"
    ".Lstub_q26_%=:\n"
    "s_mov_b32 s59, 32768\n"
    "s_call_b64 s[50:51], .Lslow_0_%=\n"
    "s_cbranch_scc1 .Lret_q26_%=\n"
    "s_call_b64 s[50:51], .Lresc_25_%=\n"
    "v_mov_b32 v160, v128\n"
    "v_mov_b32 v161, v130\n"
    "v_mov_b32 v162, v132\n"
    "v_mov_b32 v163, v134\n"
    "v_mov_b32 v164, v136\n"
    "v_mov_b32 v165, v138\n"
    "v_mov_b32 v166, v140\n"
    "v_mov_b32 v167, v142\n"
    "s_nop 1\n"
    "s_branch .Lret_q26_%=\n"
    ".Lstub_q27_%=:\n"
    "s_mov_b32 s59, 49152\n"
    "s_call_b64 s[50:51], .Lslow_1_%=\n"
    "s_cbranch_scc1 .Lret_q27_%=\n"
    "s_call_b64 s[50:51], .Lresc_26_%=\n"
    "v_mov_b32 v184, v144\n"
    "v_mov_b32 v185, v146\n"
    "v_mov_b32 v186, v148\n"
    "v_mov_b32 v187, v150\n"
    "v_mov_b32 v188, v152\n"
    "v_mov_b32 v189, v154\n"
    "v_mov_b32 v190, v156\n"
    "v_mov_b32 v191, v158\n"
    "s_nop 1\n"
    "s_branch .Lret_q27_%=\n"
    ".Lstub_q28_%=:\n"
    "s_mov_b32 s59, 0\n"
    "s_call_b64 s[50:51], .Lslow_0_%=\n"
    "s_cbranch_scc1 .Lret_q28_%=\n"
    "s_call_b64 s[50:51], .Lresc_27_%=\n"
    "v_mov_b32 v232, v128\n"
    "v_mov_b32 v233, v130\n"
    "v_mov_b32 v234, v132\n"
    "v_mov_b32 v235, v134\n"
    "v_mov_b32 v236, v136\n"
    "v_mov_b32 v237, v138\n"
    "v_mov_b32 v238, v140\n"
    "v_mov_b32 v239, v142\n"
    "s_nop 1\n"
    "s_branch .Lret_q28_%=\n"
    ".Lstub_q29_%=:\n"
    "s_mov_b32 s59, 16384\n"
    "s_call_b64 s[50:51], .Lslow_1_%=\n"
    "s_cbranch_scc1 .Lret_q29_%=\n"
    "s_call_b64 s[50:51], .Lresc_28_%=\n"
    "v_mov_b32 v240, v144\n"
    "v_mov_b32 v241, v146\n"
    "v_mov_b32 v242, v148\n"
    "v_mov_b32 v243, v150\n"
    "v_mov_b32 v244, v152\n"
    "v_mov_b32 v245, v154\n"
    "v_mov_b32 v246, v156\n"
    "v_mov_b32 v247, v158\n"
    "s_nop 1\n"
    "s_branch .Lret_q29_%=\n"
    ".Lstub_q30_%=:\n"
    "s_mov_b32 s59, 32768\n"
    "s_call_b64 s[50:51], .Lslow_0_%=\n"
    "s_cbranch_scc1 .Lret_q30_%=\n"
    "s_call_b64 s[50:51], .Lresc_29_%=\n"
    "v_mov_b32 v212, v128\n"
    "v_mov_b32 v213, v130\n"
    "v_mov_b32 v214, v132\n"
    "v_mov_b32 v215, v134\n"
    "v_mov_b32 v216, v136\n"
    "v_mov_b32 v217, v138\n"
    "v_mov_b32 v218, v140\n"
    "v_mov_b32 v219, v142\n"
    "s_nop 1\n"
    "s_branch .Lret_q30_%=\n"
    ".Lstub_bl_%=:\n"
    "s_mov_b32 s59, 49152\n"
    "s_call_b64 s[50:51], .Lslow_1_%=\n"
    "s_cbranch_scc1 .Lret_bl_%=\n"
    "s_call_b64 s[50:51], .Lresc_30_%=\n"
    "v_mov_b32 v128, v144\n"
    "v_mov_b32 v129, v146\n"
    "v_mov_b32 v130, v148\n"
    "v_mov_b32 v131, v150\n"
    "v_mov_b32 v132, v152\n"
    "v_mov_b32 v133, v154\n"
    "v_mov_b32 v134, v156\n"
    "v_mov_b32 v135, v158\n"
    "s_nop 1\n"
    "s_branch .Lret_bl_%=\n"
    ".Lslow_0_%=:\n"
    "s_and_b32 s53, s80, 15\n"
    "s_lshr_b64 s[80:81], s[80:81], 4\n"
    "s_cmp_gt_u32 s80, 1\n"
    "s_cselect_b32 s47, 0xbf800000, 0x47800000\n"
    ".Ldl_0_%=:\n"
    "s_sub_u32 s53, s53, 1\n"
    "s_cbranch_scc1 .Ldx_0_%=\n"
    "s_sleep 1\n"
    "s_branch .Ldl_0_%=\n"
    ".Ldx_0_%=:\n"
    "v_cmp_nge_f32 vcc, 0x47800000, v224\n"
    "s_cbranch_vccnz .Lreal_0_%=\n"
    "s_setpc_b64 s[50:51]\n"
    ".Lreal_0_%=:\n"
    "s_nop 15\n"
    "v_add_u32 v176, s59, v200\n"
    "v_add_u32 v177, s59, v201\n"
    "v_add_u32 v178, s59, v202\n"
    "v_add_u32 v179, s59, v203\n"
    "ds_read_b128 a[192:195], v176 offset:0\n"
    "ds_read_b128 a[196:199], v177 offset:0\n"
    "ds_read_b128 a[200:203], v178 offset:0\n"
    "ds_read_b128 a[204:207], v179 offset:0\n"
    "ds_read_b128 a[208:211], v176 offset:256\n"
    "ds_read_b128 a[212:215], v177 offset:256\n"
    "ds_read_b128 a[216:219], v178 offset:256\n"
    "ds_read_b128 a[220:223], v179 offset:256\n"
    "ds_read_b128 a[224:227], v176 offset:8192\n"
    "ds_read_b128 a[228:231], v177 offset:8192\n"
    "ds_read_b128 a[232:235], v178 offset:8192\n"
    "ds_read_b128 a[236:239], v179 offset:8192\n"
    "ds_read_b128 a[240:243], v176 offset:8448\n"
    "ds_read_b128 a[244:247], v177 offset:8448\n"
    "ds_read_b128 a[248:251], v178 offset:8448\n"
    "ds_read_b128 a[252:255], v179 offset:8448\n"
    "s_waitcnt lgkmcnt(0)\n"
    "v_mfma_f32_16x16x32_bf16 v[128:131], a[192:195], a[0:3], 0\n"
    "v_mfma_f32_16x16x32_bf16 v[128:131], a[196:199], a[4:7], v[128:131]\n"
    "v_mfma_f32_16x16x32_bf16 v[128:131], a[200:203], a[8:11], v[128:131]\n"
    "v_mfma_f32_16x16x32_bf16 v[128:131], a[204:207], a[12:15], v[128:131]\n"
    "v_mfma_f32_16x16x32_bf16 v[128:131], a[208:211], a[16:19], v[128:131]\n"
    "v_mfma_f32_16x16x32_bf16 v[128:131], a[212:215], a[20:23], v[128:131]\n"
    "v_mfma_f32_16x16x32_bf16 v[128:131], a[216:219], a[24:27], v[128:131]\n"
    "v_mfma_f32_16x16x32_bf16 v[128:131], a[220:223], a[28:31], v[128:131]\n"
    "v_mfma_f32_16x16x32_bf16 v[136:139], a[220:223], a[60:63], 0\n"
    "v_mfma_f32_16x16x32_bf16 v[136:139], a[216:219], a[56:59], v[136:139]\n"
    "v_mfma_f32_16x16x32_bf16 v[136:139], a[212:215], a[52:55], v[136:139]\n"
    "v_mfma_f32_16x16x32_bf16 v[136:139], a[208:211], a[48:51], v[136:139]\n"
    "v_mfma_f32_16x16x32_bf16 v[136:139], a[204:207], a[44:47], v[136:139]\n"
    "v_mfma_f32_16x16x32_bf16 v[136:139], a[200:203], a[40:43], v[136:139]\n"
    "v_mfma_f32_16x16x32_bf16 v[136:139], a[196:199], a[36:39], v[136:139]\n"
    "v_mfma_f32_16x16x32_bf16 v[136:139], a[192:195], a[32:35], v[136:139]\n"
    "v_mfma_f32_16x16x32_bf16 v[140:143], a[224:227], a[32:35], 0\n"
    "v_mfma_f32_16x16x32_bf16 v[140:143], a[228:231], a[36:39], v[140:143]\n"
    "v_mfma_f32_16x16x32_bf16 v[140:143], a[232:235], a[40:43], v[140:143]\n"
    "v_mfma_f32_16x16x32_bf16 v[140:143], a[236:239], a[44:47], v[140:143]\n"
    "v_mfma_f32_16x16x32_bf16 v[140:143], a[240:243], a[48:51], v[140:143]\n"
    "v_mfma_f32_16x16x32_bf16 v[140:143], a[244:247], a[52:55], v[140:143]\n"
    "v_mfma_f32_16x16x32_bf16 v[140:143], a[248:251], a[56:59], v[140:143]\n"
    "v_mfma_f32_16x16x32_bf16 v[140:143], a[252:255], a[60:63], v[140:143]\n"
    "v_mfma_f32_16x16x32_bf16 v[132:135], a[252:255], a[28:31], 0\n"
    "v_mfma_f32_16x16x32_bf16 v[132:135], a[248:251], a[24:27], v[132:135]\n"
    "v_mfma_f32_16x16x32_bf16 v[132:135], a[244:247], a[20:23], v[132:135]\n"
    "v_mfma_f32_16x16x32_bf16 v[132:135], a[240:243], a[16:19], v[132:135]\n"
    "v_mfma_f32_16x16x32_bf16 v[132:135], a[236:239], a[12:15], v[132:135]\n"
    "v_mfma_f32_16x16x32_bf16 v[132:135], a[232:235], a[8:11], v[132:135]\n"
    "v_mfma_f32_16x16x32_bf16 v[132:135], a[228:231], a[4:7], v[132:135]\n"
    "v_mfma_f32_16x16x32_bf16 v[132:135], a[224:227], a[0:3], v[132:135]\n"
    "s_nop 15\n"
    "v_max3_f32 v180, v128, v129, v130\n"
    "v_max3_f32 v181, v136, v137, v138\n"
    "v_max3_f32 v180, v180, v131, v132\n"
    "v_max3_f32 v181, v181, v139, v140\n"
    "v_max3_f32 v180, v180, v133, v134\n"
    "v_max3_f32 v181, v181, v141, v142\n"
    "v_max_f32 v180, v180, v135\n"
    "v_max_f32 v181, v181, v143\n"
    "s_nop 1\n"
    "v_permlane32_swap_b32 v180, v181\n"
    "v_max_f32 v180, v180, v181\n"
    "v_mov_b32 v182, v180\n"
    "s_nop 1\n"
    "v_permlane16_swap_b32 v180, v182\n"
    "v_max_f32 v180, v180, v182\n"
    "v_mov_b32 v181, v180\n"
    "s_nop 1\n"
    "v_permlane32_swap_b32 v180, v181\n"
    "v_sub_f32 v180, 0, v180\n"
    "v_min_f32 v180, v180, v192\n"
    "v_sub_f32 v250, v180, v192\n"
    "v_exp_f32 v250, v250\n"
    "v_mov_b32 v192, v180\n"
    "v_mov_b32 v193, v180\n"
    "v_mov_b32 v194, v180\n"
    "v_mov_b32 v195, v180\n"
    "v_mul_f32 v220, v250, v220\n"
    "v_sub_f32 v181, 0, v181\n"
    "v_min_f32 v181, v181, v196\n"
    "v_sub_f32 v251, v181, v196\n"
    "v_exp_f32 v251, v251\n"
    "v_mov_b32 v196, v181\n"
    "v_mov_b32 v197, v181\n"
    "v_mov_b32 v198, v181\n"
    "v_mov_b32 v199, v181\n"
    "v_mul_f32 v221, v251, v221\n"
    "v_and_b32 v224, 48, v225\n"
    "ds_bpermute_b32 v176, v224, v250 offset:0\n"
    "ds_bpermute_b32 v177, v224, v250 offset:4\n"
    "ds_bpermute_b32 v178, v224, v250 offset:8\n"
    "ds_bpermute_b32 v179, v224, v250 offset:12\n"
    "ds_bpermute_b32 v180, v224, v251 offset:0\n"
    "ds_bpermute_b32 v181, v224, v251 offset:4\n"
    "ds_bpermute_b32 v182, v224, v251 offset:8\n"
    "ds_bpermute_b32 v183, v224, v251 offset:12\n"
    "s_waitcnt lgkmcnt(0)\n"
    "v_mul_f32 v0, v176, v0\n"
    "v_mul_f32 v1, v177, v1\n"
    "v_mul_f32 v2, v178, v2\n"
    "v_mul_f32 v3, v179, v3\n"
    "v_mul_f32 v4, v180, v4\n"
    "v_mul_f32 v5, v181, v5\n"
    "v_mul_f32 v6, v182, v6\n"
    "v_mul_f32 v7, v183, v7\n"
    "v_mul_f32 v8, v176, v8\n"
    "v_mul_f32 v9, v177, v9\n"
    "v_mul_f32 v10, v178, v10\n"
    "v_mul_f32 v11, v179, v11\n"
    "v_mul_f32 v12, v180, v12\n"
    "v_mul_f32 v13, v181, v13\n"
    "v_mul_f32 v14, v182, v14\n"
    "v_mul_f32 v15, v183, v15\n"
    "v_mul_f32 v16, v176, v16\n"
    "v_mul_f32 v17, v177, v17\n"
    "v_mul_f32 v18, v178, v18\n"
    "v_mul_f32 v19, v179, v19\n"
    "v_mul_f32 v20, v180, v20\n"
    "v_mul_f32 v21, v181, v21\n"
    "v_mul_f32 v22, v182, v22\n"
    "v_mul_f32 v23, v183, v23\n"
    "v_mul_f32 v24, v176, v24\n"
    "v_mul_f32 v25, v177, v25\n"
    "v_mul_f32 v26, v178, v26\n"
    "v_mul_f32 v27, v179, v27\n"
    "v_mul_f32 v28, v180, v28\n"
    "v_mul_f32 v29, v181, v29\n"
    "v_mul_f32 v30, v182, v30\n"
    "v_mul_f32 v31, v183, v31\n"
    "v_mul_f32 v32, v176, v32\n"
    "v_mul_f32 v33, v177, v33\n"
    "v_mul_f32 v34, v178, v34\n"
    "v_mul_f32 v35, v179, v35\n"
    "v_mul_f32 v36, v180, v36\n"
    "v_mul_f32 v37, v181, v37\n"
    "v_mul_f32 v38, v182, v38\n"
    "v_mul_f32 v39, v183, v39\n"
    "v_mul_f32 v40, v176, v40\n"
    "v_mul_f32 v41, v177, v41\n"
    "v_mul_f32 v42, v178, v42\n"
    "v_mul_f32 v43, v179, v43\n"
    "v_mul_f32 v44, v180, v44\n"
    "v_mul_f32 v45, v181, v45\n"
    "v_mul_f32 v46, v182, v46\n"
    "v_mul_f32 v47, v183, v47\n"
    "v_mul_f32 v48, v176, v48\n"
    "v_mul_f32 v49, v177, v49\n"
    "v_mul_f32 v50, v178, v50\n"
    "v_mul_f32 v51, v179, v51\n"
    "v_mul_f32 v52, v180, v52\n"
    "v_mul_f32 v53, v181, v53\n"
    "v_mul_f32 v54, v182, v54\n"
    "v_mul_f32 v55, v183, v55\n"
    "v_mul_f32 v56, v176, v56\n"
    "v_mul_f32 v57, v177, v57\n"
    "v_mul_f32 v58, v178, v58\n"
    "v_mul_f32 v59, v179, v59\n"
    "v_mul_f32 v60, v180, v60\n"
    "v_mul_f32 v61, v181, v61\n"
    "v_mul_f32 v62, v182, v62\n"
    "v_mul_f32 v63, v183, v63\n"
    "v_mul_f32 v64, v176, v64\n"
    "v_mul_f32 v65, v177, v65\n"
    "v_mul_f32 v66, v178, v66\n"
    "v_mul_f32 v67, v179, v67\n"
    "v_mul_f32 v68, v180, v68\n"
    "v_mul_f32 v69, v181, v69\n"
    "v_mul_f32 v70, v182, v70\n"
    "v_mul_f32 v71, v183, v71\n"
    "v_mul_f32 v72, v176, v72\n"
    "v_mul_f32 v73, v177, v73\n"
    "v_mul_f32 v74, v178, v74\n"
    "v_mul_f32 v75, v179, v75\n"
    "v_mul_f32 v76, v180, v76\n"
    "v_mul_f32 v77, v181, v77\n"
    "v_mul_f32 v78, v182, v78\n"
    "v_mul_f32 v79, v183, v79\n"
    "v_mul_f32 v80, v176, v80\n"
    "v_mul_f32 v81, v177, v81\n"
    "v_mul_f32 v82, v178, v82\n"
    "v_mul_f32 v83, v179, v83\n"
    "v_mul_f32 v84, v180, v84\n"
    "v_mul_f32 v85, v181, v85\n"
    "v_mul_f32 v86, v182, v86\n"
    "v_mul_f32 v87, v183, v87\n"
    "v_mul_f32 v88, v176, v88\n"
    "v_mul_f32 v89, v177, v89\n"
    "v_mul_f32 v90, v178, v90\n"
    "v_mul_f32 v91, v179, v91\n"
    "v_mul_f32 v92, v180, v92\n"
    "v_mul_f32 v93, v181, v93\n"
    "v_mul_f32 v94, v182, v94\n"
    "v_mul_f32 v95, v183, v95\n"
    "v_mul_f32 v96, v176, v96\n"
    "v_mul_f32 v97, v177, v97\n"
    "v_mul_f32 v98, v178, v98\n"
    "v_mul_f32 v99, v179, v99\n"
    "v_mul_f32 v100, v180, v100\n"
    "v_mul_f32 v101, v181, v101\n"
    "v_mul_f32 v102, v182, v102\n"
    "v_mul_f32 v103, v183, v103\n"
    "v_mul_f32 v104, v176, v104\n"
    "v_mul_f32 v105, v177, v105\n"
    "v_mul_f32 v106, v178, v106\n"
    "v_mul_f32 v107, v179, v107\n"
    "v_mul_f32 v108, v180, v108\n"
    "v_mul_f32 v109, v181, v109\n"
    "v_mul_f32 v110, v182, v110\n"
    "v_mul_f32 v111, v183, v111\n"
    "v_mul_f32 v112, v176, v112\n"
    "v_mul_f32 v113, v177, v113\n"
    "v_mul_f32 v114, v178, v114\n"
    "v_mul_f32 v115, v179, v115\n"
    "v_mul_f32 v116, v180, v116\n"
    "v_mul_f32 v117, v181, v117\n"
    "v_mul_f32 v118, v182, v118\n"
    "v_mul_f32 v119, v183, v119\n"
    "v_mul_f32 v120, v176, v120\n"
    "v_mul_f32 v121, v177, v121\n"
    "v_mul_f32 v122, v178, v122\n"
    "v_mul_f32 v123, v179, v123\n"
    "v_mul_f32 v124, v180, v124\n"
    "v_mul_f32 v125, v181, v125\n"
    "v_mul_f32 v126, v182, v126\n"
    "v_mul_f32 v127, v183, v127\n"
    "v_add_f32 v128, v128, v192\n"
    "v_add_f32 v129, v129, v192\n"
    "v_add_f32 v130, v130, v192\n"
    "v_add_f32 v131, v131, v192\n"
    "v_add_f32 v132, v132, v192\n"
    "v_add_f32 v133, v133, v192\n"
    "v_add_f32 v134, v134, v192\n"
    "v_add_f32 v135, v135, v192\n"
    "v_add_f32 v136, v136, v196\n"
    "v_add_f32 v137, v137, v196\n"
    "v_add_f32 v138, v138, v196\n"
    "v_add_f32 v139, v139, v196\n"
    "v_add_f32 v140, v140, v196\n"
    "v_add_f32 v141, v141, v196\n"
    "v_add_f32 v142, v142, v196\n"
    "v_add_f32 v143, v143, v196\n"
    "v_exp_f32 v176, v128\n"
    "v_exp_f32 v177, v129\n"
    "v_exp_f32 v178, v130\n"
    "v_add_f32 v222, v176, v177\n"
    "v_cvt_pk_bf16_f32 v128, v176, v177\n"
    "v_exp_f32 v179, v131\n"
    "v_add_f32 v222, v222, v178\n"
    "v_exp_f32 v180, v136\n"
    "v_add_f32 v222, v222, v179\n"
    "v_cvt_pk_bf16_f32 v130, v178, v179\n"
    "v_exp_f32 v181, v137\n"
    "v_exp_f32 v182, v138\n"
    "v_add_f32 v223, v180, v181\n"
    "v_cvt_pk_bf16_f32 v136, v180, v181\n"
    "v_exp_f32 v183, v139\n"
    "v_add_f32 v223, v223, v182\n"
    "v_exp_f32 v176, v132\n"
    "v_add_f32 v223, v223, v183\n"
    "v_cvt_pk_bf16_f32 v138, v182, v183\n"
    "v_exp_f32 v177, v133\n"
    "v_add_f32 v222, v222, v176\n"
    "v_exp_f32 v178, v134\n"
    "v_add_f32 v222, v222, v177\n"
    "v_cvt_pk_bf16_f32 v132, v176, v177\n"
    "v_exp_f32 v179, v135\n"
    "v_add_f32 v222, v222, v178\n"
    "v_exp_f32 v180, v140\n"
    "v_add_f32 v222, v222, v179\n"
    "v_cvt_pk_bf16_f32 v134, v178, v179\n"
    "v_exp_f32 v181, v141\n"
    "v_add_f32 v223, v223, v180\n"
    "v_exp_f32 v182, v142\n"
    "v_add_f32 v223, v223, v181\n"
    "v_cvt_pk_bf16_f32 v140, v180, v181\n"
    "v_exp_f32 v183, v143\n"
    "v_add_f32 v223, v223, v182\n"
    "s_nop 0\n"
    "v_add_f32 v223, v223, v183\n"
    "v_cvt_pk_bf16_f32 v142, v182, v183\n"
    "v_mfma_f32_16x16x32_bf16 v[144:147], a[64:67], a[0:3], v[192:195]\n"
    "v_mfma_f32_16x16x32_bf16 v[144:147], a[68:71], a[4:7], v[144:147]\n"
    "v_mfma_f32_16x16x32_bf16 v[144:147], a[72:75], a[8:11], v[144:147]\n"
    "v_mfma_f32_16x16x32_bf16 v[144:147], a[76:79], a[12:15], v[144:147]\n"
    "v_mfma_f32_16x16x32_bf16 v[144:147], a[80:83], a[16:19], v[144:147]\n"
    "v_mfma_f32_16x16x32_bf16 v[144:147], a[84:87], a[20:23], v[144:147]\n"
    "v_mfma_f32_16x16x32_bf16 v[144:147], a[88:91], a[24:27], v[144:147]\n"
    "v_mfma_f32_16x16x32_bf16 v[144:147], a[92:95], a[28:31], v[144:147]\n"
    "v_mfma_f32_16x16x32_bf16 v[152:155], a[92:95], a[60:63], v[196:199]\n"
    "v_mfma_f32_16x16x32_bf16 v[152:155], a[88:91], a[56:59], v[152:155]\n"
    "v_mfma_f32_16x16x32_bf16 v[152:155], a[84:87], a[52:55], v[152:155]\n"
    "v_mfma_f32_16x16x32_bf16 v[152:155], a[80:83], a[48:51], v[152:155]\n"
    "v_mfma_f32_16x16x32_bf16 v[152:155], a[76:79], a[44:47], v[152:155]\n"
    "v_mfma_f32_16x16x32_bf16 v[152:155], a[72:75], a[40:43], v[152:155]\n"
    "v_mfma_f32_16x16x32_bf16 v[152:155], a[68:71], a[36:39], v[152:155]\n"
    "v_mfma_f32_16x16x32_bf16 v[152:155], a[64:67], a[32:35], v[152:155]\n"
    "v_mfma_f32_16x16x32_bf16 v[156:159], a[96:99], a[32:35], v[196:199]\n"
    "v_mfma_f32_16x16x32_bf16 v[156:159], a[100:103], a[36:39], v[156:159]\n"
    "v_mfma_f32_16x16x32_bf16 v[156:159], a[104:107], a[40:43], v[156:159]\n"
    "v_mfma_f32_16x16x32_bf16 v[156:159], a[108:111], a[44:47], v[156:159]\n"
    "v_mfma_f32_16x16x32_bf16 v[156:159], a[112:115], a[48:51], v[156:159]\n"
    "v_mfma_f32_16x16x32_bf16 v[156:159], a[116:119], a[52:55], v[156:159]\n"
    "v_mfma_f32_16x16x32_bf16 v[156:159], a[120:123], a[56:59], v[156:159]\n"
    "v_mfma_f32_16x16x32_bf16 v[156:159], a[124:127], a[60:63], v[156:159]\n"
    "v_mfma_f32_16x16x32_bf16 v[148:151], a[124:127], a[28:31], v[192:195]\n"
    "v_mfma_f32_16x16x32_bf16 v[148:151], a[120:123], a[24:27], v[148:151]\n"
    "v_mfma_f32_16x16x32_bf16 v[148:151], a[116:119], a[20:23], v[148:151]\n"
    "s_nop 15\n"
    "s_cmp_lg_u32 s49, s49\n"
    "s_setpc_b64 s[50:51]\n"
    ".Lslow_1_%=:\n"
    "s_and_b32 s53, s80, 15\n"
    "s_lshr_b64 s[80:81], s[80:81], 4\n"
    "s_cmp_gt_u32 s80, 1\n"
    "s_cselect_b32 s47, 0xbf800000, 0x47800000\n"
    ".Ldl_1_%=:\n"
    "s_sub_u32 s53, s53, 1\n"
    "s_cbranch_scc1 .Ldx_1_%=\n"
    "s_sleep 1\n"
    "s_branch .Ldl_1_%=\n"
    ".Ldx_1_%=:\n"
    "v_cmp_nge_f32 vcc, 0x47800000, v224\n"
    "s_cbranch_vccnz .Lreal_1_%=\n"
    "s_setpc_b64 s[50:51]\n"
    ".Lreal_1_%=:\n"
    "s_nop 15\n"
    "v_add_u32 v176, s59, v200\n"
    "v_add_u32 v177, s59, v201\n"
    "v_add_u32 v178, s59, v202\n"
    "v_add_u32 v179, s59, v203\n"
    "ds_read_b128 a[192:195], v176 offset:0\n"
    "ds_read_b128 a[196:199], v177 offset:0\n"
    "ds_read_b128 a[200:203], v178 offset:0\n"
    "ds_read_b128 a[204:207], v179 offset:0\n"
    "ds_read_b128 a[208:211], v176 offset:256\n"
    "ds_read_b128 a[212:215], v177 offset:256\n"
    "ds_read_b128 a[216:219], v178 offset:256\n"
    "ds_read_b128 a[220:223], v179 offset:256\n"
    "ds_read_b128 a[224:227], v176 offset:8192\n"
    "ds_read_b128 a[228:231], v177 offset:8192\n"
    "ds_read_b128 a[232:235], v178 offset:8192\n"
    "ds_read_b128 a[236:239], v179 offset:8192\n"
    "ds_read_b128 a[240:243], v176 offset:8448\n"
    "ds_read_b128 a[244:247], v177 offset:8448\n"
    "ds_read_b128 a[248:251], v178 offset:8448\n"
    "ds_read_b128 a[252:255], v179 offset:8448\n"
    "s_waitcnt lgkmcnt(0)\n"
    "v_mfma_f32_16x16x32_bf16 v[144:147], a[192:195], a[0:3], 0\n"
    "v_mfma_f32_16x16x32_bf16 v[144:147], a[196:199], a[4:7], v[144:147]\n"
    "v_mfma_f32_16x16x32_bf16 v[144:147], a[200:203], a[8:11], v[144:147]\n"
    "v_mfma_f32_16x16x32_bf16 v[144:147], a[204:207], a[12:15], v[144:147]\n"
    "v_mfma_f32_16x16x32_bf16 v[144:147], a[208:211], a[16:19], v[144:147]\n"
    "v_mfma_f32_16x16x32_bf16 v[144:147], a[212:215], a[20:23], v[144:147]\n"
    "v_mfma_f32_16x16x32_bf16 v[144:147], a[216:219], a[24:27], v[144:147]\n"
    "v_mfma_f32_16x16x32_bf16 v[144:147], a[220:223], a[28:31], v[144:147]\n"
    "v_mfma_f32_16x16x32_bf16 v[152:155], a[220:223], a[60:63], 0\n"
    "v_mfma_f32_16x16x32_bf16 v[152:155], a[216:219], a[56:59], v[152:155]\n"
    "v_mfma_f32_16x16x32_bf16 v[152:155], a[212:215], a[52:55], v[152:155]\n"
    "v_mfma_f32_16x16x32_bf16 v[152:155], a[208:211], a[48:51], v[152:155]\n"
    "v_mfma_f32_16x16x32_bf16 v[152:155], a[204:207], a[44:47], v[152:155]\n"
    "v_mfma_f32_16x16x32_bf16 v[152:155], a[200:203], a[40:43], v[152:155]\n"
    "v_mfma_f32_16x16x32_bf16 v[152:155], a[196:199], a[36:39], v[152:155]\n"
    "v_mfma_f32_16x16x32_bf16 v[152:155], a[192:195], a[32:35], v[152:155]\n"
    "v_mfma_f32_16x16x32_bf16 v[156:159], a[224:227], a[32:35], 0\n"
    "v_mfma_f32_16x16x32_bf16 v[156:159], a[228:231], a[36:39], v[156:159]\n"
    "v_mfma_f32_16x16x32_bf16 v[156:159], a[232:235], a[40:43], v[156:159]\n"
    "v_mfma_f32_16x16x32_bf16 v[156:159], a[236:239], a[44:47], v[156:159]\n"
    "v_mfma_f32_16x16x32_bf16 v[156:159], a[240:243], a[48:51], v[156:159]\n"
    "v_mfma_f32_16x16x32_bf16 v[156:159], a[244:247], a[52:55], v[156:159]\n"
    "v_mfma_f32_16x16x32_bf16 v[156:159], a[248:251], a[56:59], v[156:159]\n"
    "v_mfma_f32_16x16x32_bf16 v[156:159], a[252:255], a[60:63], v[156:159]\n"
    "v_mfma_f32_16x16x32_bf16 v[148:151], a[252:255], a[28:31], 0\n"
    "v_mfma_f32_16x16x32_bf16 v[148:151], a[248:251], a[24:27], v[148:151]\n"
    "v_mfma_f32_16x16x32_bf16 v[148:151], a[244:247], a[20:23], v[148:151]\n"
    "v_mfma_f32_16x16x32_bf16 v[148:151], a[240:243], a[16:19], v[148:151]\n"
    "v_mfma_f32_16x16x32_bf16 v[148:151], a[236:239], a[12:15], v[148:151]\n"
    "v_mfma_f32_16x16x32_bf16 v[148:151], a[232:235], a[8:11], v[148:151]\n"
    "v_mfma_f32_16x16x32_bf16 v[148:151], a[228:231], a[4:7], v[148:151]\n"
    "v_mfma_f32_16x16x32_bf16 v[148:151], a[224:227], a[0:3], v[148:151]\n"
    "s_nop 15\n"
    "v_max3_f32 v180, v144, v145, v146\n"
    "v_max3_f32 v181, v152, v153, v154\n"
    "v_max3_f32 v180, v180, v147, v148\n"
    "v_max3_f32 v181, v181, v155, v156\n"
    "v_max3_f32 v180, v180, v149, v150\n"
    "v_max3_f32 v181, v181, v157, v158\n"
    "v_max_f32 v180, v180, v151\n"
    "v_max_f32 v181, v181, v159\n"
    "s_nop 1\n"
    "v_permlane32_swap_b32 v180, v181\n"
    "v_max_f32 v180, v180, v181\n"
    "v_mov_b32 v182, v180\n"
    "s_nop 1\n"
    "v_permlane16_swap_b32 v180, v182\n"
    "v_max_f32 v180, v180, v182\n"
    "v_mov_b32 v181, v180\n"
    "s_nop 1\n"
    "v_permlane32_swap_b32 v180, v181\n"
    "v_sub_f32 v180, 0, v180\n"
    "v_min_f32 v180, v180, v192\n"
    "v_sub_f32 v250, v180, v192\n"
    "v_exp_f32 v250, v250\n"
    "v_mov_b32 v192, v180\n"
    "v_mov_b32 v193, v180\n"
    "v_mov_b32 v194, v180\n"
    "v_mov_b32 v195, v180\n"
    "v_mul_f32 v220, v250, v220\n"
    "v_sub_f32 v181, 0, v181\n"
    "v_min_f32 v181, v181, v196\n"
    "v_sub_f32 v251, v181, v196\n"
    "v_exp_f32 v251, v251\n"
    "v_mov_b32 v196, v181\n"
    "v_mov_b32 v197, v181\n"
    "v_mov_b32 v198, v181\n"
    "v_mov_b32 v199, v181\n"
    "v_mul_f32 v221, v251, v221\n"
    "v_and_b32 v224, 48, v225\n"
    "ds_bpermute_b32 v176, v224, v250 offset:0\n"
    "ds_bpermute_b32 v177, v224, v250 offset:4\n"
    "ds_bpermute_b32 v178, v224, v250 offset:8\n"
    "ds_bpermute_b32 v179, v224, v250 offset:12\n"
    "ds_bpermute_b32 v180, v224, v251 offset:0\n"
    "ds_bpermute_b32 v181, v224, v251 offset:4\n"
    "ds_bpermute_b32 v182, v224, v251 offset:8\n"
    "ds_bpermute_b32 v183, v224, v251 offset:12\n"
    "s_waitcnt lgkmcnt(0)\n"
    "v_mul_f32 v0, v176, v0\n"
    "v_mul_f32 v1, v177, v1\n"
    "v_mul_f32 v2, v178, v2\n"
    "v_mul_f32 v3, v179, v3\n"
    "v_mul_f32 v4, v180, v4\n"
    "v_mul_f32 v5, v181, v5\n"
    "v_mul_f32 v6, v182, v6\n"
    "v_mul_f32 v7, v183, v7\n"
    "v_mul_f32 v8, v176, v8\n"
    "v_mul_f32 v9, v177, v9\n"
    "v_mul_f32 v10, v178, v10\n"
    "v_mul_f32 v11, v179, v11\n"
    "v_mul_f32 v12, v180, v12\n"
    "v_mul_f32 v13, v181, v13\n"
    "v_mul_f32 v14, v182, v14\n"
    "v_mul_f32 v15, v183, v15\n"
    "v_mul_f32 v16, v176, v16\n"
    "v_mul_f32 v17, v177, v17\n"
    "v_mul_f32 v18, v178, v18\n"
    "v_mul_f32 v19, v179, v19\n"
    "v_mul_f32 v20, v180, v20\n"
    "v_mul_f32 v21, v181, v21\n"
    "v_mul_f32 v22, v182, v22\n"
    "v_mul_f32 v23, v183, v23\n"
    "v_mul_f32 v24, v176, v24\n"
    "v_mul_f32 v25, v177, v25\n"
    "v_mul_f32 v26, v178, v26\n"
    "v_mul_f32 v27, v179, v27\n"
    "v_mul_f32 v28, v180, v28\n"
    "v_mul_f32 v29, v181, v29\n"
    "v_mul_f32 v30, v182, v30\n"
    "v_mul_f32 v31, v183, v31\n"
    "v_mul_f32 v32, v176, v32\n"
    "v_mul_f32 v33, v177, v33\n"
    "v_mul_f32 v34, v178, v34\n"
    "v_mul_f32 v35, v179, v35\n"
    "v_mul_f32 v36, v180, v36\n"
    "v_mul_f32 v37, v181, v37\n"
    "v_mul_f32 v38, v182, v38\n"
    "v_mul_f32 v39, v183, v39\n"
    "v_mul_f32 v40, v176, v40\n"
    "v_mul_f32 v41, v177, v41\n"
    "v_mul_f32 v42, v178, v42\n"
    "v_mul_f32 v43, v179, v43\n"
    "v_mul_f32 v44, v180, v44\n"
    "v_mul_f32 v45, v181, v45\n"
    "v_mul_f32 v46, v182, v46\n"
    "v_mul_f32 v47, v183, v47\n"
    "v_mul_f32 v48, v176, v48\n"
    "v_mul_f32 v49, v177, v49\n"
    "v_mul_f32 v50, v178, v50\n"
    "v_mul_f32 v51, v179, v51\n"
    "v_mul_f32 v52, v180, v52\n"
    "v_mul_f32 v53, v181, v53\n"
    "v_mul_f32 v54, v182, v54\n"
    "v_mul_f32 v55, v183, v55\n"
    "v_mul_f32 v56, v176, v56\n"
    "v_mul_f32 v57, v177, v57\n"
    "v_mul_f32 v58, v178, v58\n"
    "v_mul_f32 v59, v179, v59\n"
    "v_mul_f32 v60, v180, v60\n"
    "v_mul_f32 v61, v181, v61\n"
    "v_mul_f32 v62, v182, v62\n"
    "v_mul_f32 v63, v183, v63\n"
    "v_mul_f32 v64, v176, v64\n"
    "v_mul_f32 v65, v177, v65\n"
    "v_mul_f32 v66, v178, v66\n"
    "v_mul_f32 v67, v179, v67\n"
    "v_mul_f32 v68, v180, v68\n"
    "v_mul_f32 v69, v181, v69\n"
    "v_mul_f32 v70, v182, v70\n"
    "v_mul_f32 v71, v183, v71\n"
    "v_mul_f32 v72, v176, v72\n"
    "v_mul_f32 v73, v177, v73\n"
    "v_mul_f32 v74, v178, v74\n"
    "v_mul_f32 v75, v179, v75\n"
    "v_mul_f32 v76, v180, v76\n"
    "v_mul_f32 v77, v181, v77\n"
    "v_mul_f32 v78, v182, v78\n"
    "v_mul_f32 v79, v183, v79\n"
    "v_mul_f32 v80, v176, v80\n"
    "v_mul_f32 v81, v177, v81\n"
    "v_mul_f32 v82, v178, v82\n"
    "v_mul_f32 v83, v179, v83\n"
    "v_mul_f32 v84, v180, v84\n"
    "v_mul_f32 v85, v181, v85\n"
    "v_mul_f32 v86, v182, v86\n"
    "v_mul_f32 v87, v183, v87\n"
    "v_mul_f32 v88, v176, v88\n"
    "v_mul_f32 v89, v177, v89\n"
    "v_mul_f32 v90, v178, v90\n"
    "v_mul_f32 v91, v179, v91\n"
    "v_mul_f32 v92, v180, v92\n"
    "v_mul_f32 v93, v181, v93\n"
    "v_mul_f32 v94, v182, v94\n"
    "v_mul_f32 v95, v183, v95\n"
    "v_mul_f32 v96, v176, v96\n"
    "v_mul_f32 v97, v177, v97\n"
    "v_mul_f32 v98, v178, v98\n"
    "v_mul_f32 v99, v179, v99\n"
    "v_mul_f32 v100, v180, v100\n"
    "v_mul_f32 v101, v181, v101\n"
    "v_mul_f32 v102, v182, v102\n"
    "v_mul_f32 v103, v183, v103\n"
    "v_mul_f32 v104, v176, v104\n"
    "v_mul_f32 v105, v177, v105\n"
    "v_mul_f32 v106, v178, v106\n"
    "v_mul_f32 v107, v179, v107\n"
    "v_mul_f32 v108, v180, v108\n"
    "v_mul_f32 v109, v181, v109\n"
    "v_mul_f32 v110, v182, v110\n"
    "v_mul_f32 v111, v183, v111\n"
    "v_mul_f32 v112, v176, v112\n"
    "v_mul_f32 v113, v177, v113\n"
    "v_mul_f32 v114, v178, v114\n"
    "v_mul_f32 v115, v179, v115\n"
    "v_mul_f32 v116, v180, v116\n"
    "v_mul_f32 v117, v181, v117\n"
    "v_mul_f32 v118, v182, v118\n"
    "v_mul_f32 v119, v183, v119\n"
    "v_mul_f32 v120, v176, v120\n"
    "v_mul_f32 v121, v177, v121\n"
    "v_mul_f32 v122, v178, v122\n"
    "v_mul_f32 v123, v179, v123\n"
    "v_mul_f32 v124, v180, v124\n"
    "v_mul_f32 v125, v181, v125\n"
    "v_mul_f32 v126, v182, v126\n"
    "v_mul_f32 v127, v183, v127\n"
    "v_add_f32 v144, v144, v192\n"
    "v_add_f32 v145, v145, v192\n"
    "v_add_f32 v146, v146, v192\n"
    "v_add_f32 v147, v147, v192\n"
    "v_add_f32 v148, v148, v192\n"
    "v_add_f32 v149, v149, v192\n"
    "v_add_f32 v150, v150, v192\n"
    "v_add_f32 v151, v151, v192\n"
    "v_add_f32 v152, v152, v196\n"
    "v_add_f32 v153, v153, v196\n"
    "v_add_f32 v154, v154, v196\n"
    "v_add_f32 v155, v155, v196\n"
    "v_add_f32 v156, v156, v196\n"
    "v_add_f32 v157, v157, v196\n"
    "v_add_f32 v158, v158, v196\n"
    "v_add_f32 v159, v159, v196\n"
    "v_exp_f32 v176, v144\n"
    "v_exp_f32 v177, v145\n"
    "v_exp_f32 v178, v146\n"
    "v_add_f32 v222, v176, v177\n"
    "v_cvt_pk_bf16_f32 v144, v176, v177\n"
    "v_exp_f32 v179, v147\n"
    "v_add_f32 v222, v222, v178\n"
    "v_exp_f32 v180, v152\n"
    "v_add_f32 v222, v222, v179\n"
    "v_cvt_pk_bf16_f32 v146, v178, v179\n"
    "v_exp_f32 v181, v153\n"
    "v_exp_f32 v182, v154\n"
    "v_add_f32 v223, v180, v181\n"
    "v_cvt_pk_bf16_f32 v152, v180, v181\n"
    "v_exp_f32 v183, v155\n"
    "v_add_f32 v223, v223, v182\n"
    "v_exp_f32 v176, v148\n"
    "v_add_f32 v223, v223, v183\n"
    "v_cvt_pk_bf16_f32 v154, v182, v183\n"
    "v_exp_f32 v177, v149\n"
    "v_add_f32 v222, v222, v176\n"
    "v_exp_f32 v178, v150\n"
    "v_add_f32 v222, v222, v177\n"
    "v_cvt_pk_bf16_f32 v148, v176, v177\n"
    "v_exp_f32 v179, v151\n"
    "v_add_f32 v222, v222, v178\n"
    "v_exp_f32 v180, v156\n"
    "v_add_f32 v222, v222, v179\n"
    "v_cvt_pk_bf16_f32 v150, v178, v179\n"
    "v_exp_f32 v181, v157\n"
    "v_add_f32 v223, v223, v180\n"
    "v_exp_f32 v182, v158\n"
    "v_add_f32 v223, v223, v181\n"
    "v_cvt_pk_bf16_f32 v156, v180, v181\n"
    "v_exp_f32 v183, v159\n"
    "v_add_f32 v223, v223, v182\n"
    "s_nop 0\n"
    "v_add_f32 v223, v223, v183\n"
    "v_cvt_pk_bf16_f32 v158, v182, v183\n"
    "v_mfma_f32_16x16x32_bf16 v[128:131], a[64:67], a[0:3], v[192:195]\n"
    "v_mfma_f32_16x16x32_bf16 v[128:131], a[68:71], a[4:7], v[128:131]\n"
    "v_mfma_f32_16x16x32_bf16 v[128:131], a[72:75], a[8:11], v[128:131]\n"
    "v_mfma_f32_16x16x32_bf16 v[128:131], a[76:79], a[12:15], v[128:131]\n"
    "v_mfma_f32_16x16x32_bf16 v[128:131], a[80:83], a[16:19], v[128:131]\n"
    "v_mfma_f32_16x16x32_bf16 v[128:131], a[84:87], a[20:23], v[128:131]\n"
    "v_mfma_f32_16x16x32_bf16 v[128:131], a[88:91], a[24:27], v[128:131]\n"
    "v_mfma_f32_16x16x32_bf16 v[128:131], a[92:95], a[28:31], v[128:131]\n"
    "v_mfma_f32_16x16x32_bf16 v[136:139], a[92:95], a[60:63], v[196:199]\n"
    "v_mfma_f32_16x16x32_bf16 v[136:139], a[88:91], a[56:59], v[136:139]\n"
    "v_mfma_f32_16x16x32_bf16 v[136:139], a[84:87], a[52:55], v[136:139]\n"
    "v_mfma_f32_16x16x32_bf16 v[136:139], a[80:83], a[48:51], v[136:139]\n"
    "v_mfma_f32_16x16x32_bf16 v[136:139], a[76:79], a[44:47], v[136:139]\n"
    "v_mfma_f32_16x16x32_bf16 v[136:139], a[72:75], a[40:43], v[136:139]\n"
    "v_mfma_f32_16x16x32_bf16 v[136:139], a[68:71], a[36:39], v[136:139]\n"
    "v_mfma_f32_16x16x32_bf16 v[136:139], a[64:67], a[32:35], v[136:139]\n"
    "v_mfma_f32_16x16x32_bf16 v[140:143], a[96:99], a[32:35], v[196:199]\n"
    "v_mfma_f32_16x16x32_bf16 v[140:143], a[100:103], a[36:39], v[140:143]\n"
    "v_mfma_f32_16x16x32_bf16 v[140:143], a[104:107], a[40:43], v[140:143]\n"
    "v_mfma_f32_16x16x32_bf16 v[140:143], a[108:111], a[44:47], v[140:143]\n"
    "v_mfma_f32_16x16x32_bf16 v[140:143], a[112:115], a[48:51], v[140:143]\n"
    "v_mfma_f32_16x16x32_bf16 v[140:143], a[116:119], a[52:55], v[140:143]\n"
    "v_mfma_f32_16x16x32_bf16 v[140:143], a[120:123], a[56:59], v[140:143]\n"
    "v_mfma_f32_16x16x32_bf16 v[140:143], a[124:127], a[60:63], v[140:143]\n"
    "v_mfma_f32_16x16x32_bf16 v[132:135], a[124:127], a[28:31], v[192:195]\n"
    "v_mfma_f32_16x16x32_bf16 v[132:135], a[120:123], a[24:27], v[132:135]\n"
    "v_mfma_f32_16x16x32_bf16 v[132:135], a[116:119], a[20:23], v[132:135]\n"
    "s_nop 15\n"
    "s_cmp_lg_u32 s49, s49\n"
    "s_setpc_b64 s[50:51]\n"
    ".Lresc_30_%=:\n"
    "v_lshlrev_b32 v176, 16, v212\n"
    "v_and_b32 v177, 0xffff0000, v212\n"
    "v_mul_f32 v176, v250, v176\n"
    "v_mul_f32 v177, v250, v177\n"
    "v_cvt_pk_bf16_f32 v212, v176, v177\n"
    "v_lshlrev_b32 v176, 16, v213\n"
    "v_and_b32 v177, 0xffff0000, v213\n"
    "v_mul_f32 v176, v250, v176\n"
    "v_mul_f32 v177, v250, v177\n"
    "v_cvt_pk_bf16_f32 v213, v176, v177\n"
    "v_lshlrev_b32 v176, 16, v214\n"
    "v_and_b32 v177, 0xffff0000, v214\n"
    "v_mul_f32 v176, v250, v176\n"
    "v_mul_f32 v177, v250, v177\n"
    "v_cvt_pk_bf16_f32 v214, v176, v177\n"
    "v_lshlrev_b32 v176, 16, v215\n"
    "v_and_b32 v177, 0xffff0000, v215\n"
    "v_mul_f32 v176, v250, v176\n"
    "v_mul_f32 v177, v250, v177\n"
    "v_cvt_pk_bf16_f32 v215, v176, v177\n"
    "v_lshlrev_b32 v176, 16, v216\n"
    "v_and_b32 v177, 0xffff0000, v216\n"
    "v_mul_f32 v176, v251, v176\n"
    "v_mul_f32 v177, v251, v177\n"
    "v_cvt_pk_bf16_f32 v216, v176, v177\n"
    "v_lshlrev_b32 v176, 16, v217\n"
    "v_and_b32 v177, 0xffff0000, v217\n"
    "v_mul_f32 v176, v251, v176\n"
    "v_mul_f32 v177, v251, v177\n"
    "v_cvt_pk_bf16_f32 v217, v176, v177\n"
    "v_lshlrev_b32 v176, 16, v218\n"
    "v_and_b32 v177, 0xffff0000, v218\n"
    "v_mul_f32 v176, v251, v176\n"
    "v_mul_f32 v177, v251, v177\n"
    "v_cvt_pk_bf16_f32 v218, v176, v177\n"
    "v_lshlrev_b32 v176, 16, v219\n"
    "v_and_b32 v177, 0xffff0000, v219\n"
    "v_mul_f32 v176, v251, v176\n"
    "v_mul_f32 v177, v251, v177\n"
    "v_cvt_pk_bf16_f32 v219, v176, v177\n"
    ".Lresc_29_%=:\n"
    "v_lshlrev_b32 v176, 16, v240\n"
    "v_and_b32 v177, 0xffff0000, v240\n"
    "v_mul_f32 v176, v250, v176\n"
    "v_mul_f32 v177, v250, v177\n"
    "v_cvt_pk_bf16_f32 v240, v176, v177\n"
    "v_lshlrev_b32 v176, 16, v241\n"
    "v_and_b32 v177, 0xffff0000, v241\n"
    "v_mul_f32 v176, v250, v176\n"
    "v_mul_f32 v177, v250, v177\n"
    "v_cvt_pk_bf16_f32 v241, v176, v177\n"
    "v_lshlrev_b32 v176, 16, v242\n"
    "v_and_b32 v177, 0xffff0000, v242\n"
    "v_mul_f32 v176, v250, v176\n"
    "v_mul_f32 v177, v250, v177\n"
    "v_cvt_pk_bf16_f32 v242, v176, v177\n"
    "v_lshlrev_b32 v176, 16, v243\n"
    "v_and_b32 v177, 0xffff0000, v243\n"
    "v_mul_f32 v176, v250, v176\n"
    "v_mul_f32 v177, v250, v177\n"
    "v_cvt_pk_bf16_f32 v243, v176, v177\n"
    "v_lshlrev_b32 v176, 16, v244\n"
    "v_and_b32 v177, 0xffff0000, v244\n"
    "v_mul_f32 v176, v251, v176\n"
    "v_mul_f32 v177, v251, v177\n"
    "v_cvt_pk_bf16_f32 v244, v176, v177\n"
    "v_lshlrev_b32 v176, 16, v245\n"
    "v_and_b32 v177, 0xffff0000, v245\n"
    "v_mul_f32 v176, v251, v176\n"
    "v_mul_f32 v177, v251, v177\n"
    "v_cvt_pk_bf16_f32 v245, v176, v177\n"
    "v_lshlrev_b32 v176, 16, v246\n"
    "v_and_b32 v177, 0xffff0000, v246\n"
    "v_mul_f32 v176, v251, v176\n"
    "v_mul_f32 v177, v251, v177\n"
    "v_cvt_pk_bf16_f32 v246, v176, v177\n"
    "v_lshlrev_b32 v176, 16, v247\n"
    "v_and_b32 v177, 0xffff0000, v247\n"
    "v_mul_f32 v176, v251, v176\n"
    "v_mul_f32 v177, v251, v177\n"
    "v_cvt_pk_bf16_f32 v247, v176, v177\n"
    ".Lresc_28_%=:\n"
    "v_lshlrev_b32 v176, 16, v232\n"
    "v_and_b32 v177, 0xffff0000, v232\n"
    "v_mul_f32 v176, v250, v176\n"
    "v_mul_f32 v177, v250, v177\n"
    "v_cvt_pk_bf16_f32 v232, v176, v177\n"
    "v_lshlrev_b32 v176, 16, v233\n"
    "v_and_b32 v177, 0xffff0000, v233\n"
    "v_mul_f32 v176, v250, v176\n"
    "v_mul_f32 v177, v250, v177\n"
    "v_cvt_pk_bf16_f32 v233, v176, v177\n"
    "v_lshlrev_b32 v176, 16, v234\n"
    "v_and_b32 v177, 0xffff0000, v234\n"
    "v_mul_f32 v176, v250, v176\n"
    "v_mul_f32 v177, v250, v177\n"
    "v_cvt_pk_bf16_f32 v234, v176, v177\n"
    "v_lshlrev_b32 v176, 16, v235\n"
    "v_and_b32 v177, 0xffff0000, v235\n"
    "v_mul_f32 v176, v250, v176\n"
    "v_mul_f32 v177, v250, v177\n"
    "v_cvt_pk_bf16_f32 v235, v176, v177\n"
    "v_lshlrev_b32 v176, 16, v236\n"
    "v_and_b32 v177, 0xffff0000, v236\n"
    "v_mul_f32 v176, v251, v176\n"
    "v_mul_f32 v177, v251, v177\n"
    "v_cvt_pk_bf16_f32 v236, v176, v177\n"
    "v_lshlrev_b32 v176, 16, v237\n"
    "v_and_b32 v177, 0xffff0000, v237\n"
    "v_mul_f32 v176, v251, v176\n"
    "v_mul_f32 v177, v251, v177\n"
    "v_cvt_pk_bf16_f32 v237, v176, v177\n"
    "v_lshlrev_b32 v176, 16, v238\n"
    "v_and_b32 v177, 0xffff0000, v238\n"
    "v_mul_f32 v176, v251, v176\n"
    "v_mul_f32 v177, v251, v177\n"
    "v_cvt_pk_bf16_f32 v238, v176, v177\n"
    "v_lshlrev_b32 v176, 16, v239\n"
    "v_and_b32 v177, 0xffff0000, v239\n"
    "v_mul_f32 v176, v251, v176\n"
    "v_mul_f32 v177, v251, v177\n"
    "v_cvt_pk_bf16_f32 v239, v176, v177\n"
    ".Lresc_27_%=:\n"
    "v_lshlrev_b32 v176, 16, v184\n"
    "v_and_b32 v177, 0xffff0000, v184\n"
    "v_mul_f32 v176, v250, v176\n"
    "v_mul_f32 v177, v250, v177\n"
    "v_cvt_pk_bf16_f32 v184, v176, v177\n"
    "v_lshlrev_b32 v176, 16, v185\n"
    "v_and_b32 v177, 0xffff0000, v185\n"
    "v_mul_f32 v176, v250, v176\n"
    "v_mul_f32 v177, v250, v177\n"
    "v_cvt_pk_bf16_f32 v185, v176, v177\n"
    "v_lshlrev_b32 v176, 16, v186\n"
    "v_and_b32 v177, 0xffff0000, v186\n"
    "v_mul_f32 v176, v250, v176\n"
    "v_mul_f32 v177, v250, v177\n"
    "v_cvt_pk_bf16_f32 v186, v176, v177\n"
    "v_lshlrev_b32 v176, 16, v187\n"
    "v_and_b32 v177, 0xffff0000, v187\n"
    "v_mul_f32 v176, v250, v176\n"
    "v_mul_f32 v177, v250, v177\n"
    "v_cvt_pk_bf16_f32 v187, v176, v177\n"
    "v_lshlrev_b32 v176, 16, v188\n"
    "v_and_b32 v177, 0xffff0000, v188\n"
    "v_mul_f32 v176, v251, v176\n"
    "v_mul_f32 v177, v251, v177\n"
    "v_cvt_pk_bf16_f32 v188, v176, v177\n"
    "v_lshlrev_b32 v176, 16, v189\n"
    "v_and_b32 v177, 0xffff0000, v189\n"
    "v_mul_f32 v176, v251, v176\n"
    "v_mul_f32 v177, v251, v177\n"
    "v_cvt_pk_bf16_f32 v189, v176, v177\n"
    "v_lshlrev_b32 v176, 16, v190\n"
    "v_and_b32 v177, 0xffff0000, v190\n"
    "v_mul_f32 v176, v251, v176\n"
    "v_mul_f32 v177, v251, v177\n"
    "v_cvt_pk_bf16_f32 v190, v176, v177\n"
    "v_lshlrev_b32 v176, 16, v191\n"
    "v_and_b32 v177, 0xffff0000, v191\n"
    "v_mul_f32 v176, v251, v176\n"
    "v_mul_f32 v177, v251, v177\n"
    "v_cvt_pk_bf16_f32 v191, v176, v177\n"
    ".Lresc_26_%=:\n"
    "v_lshlrev_b32 v176, 16, v160\n"
    "v_and_b32 v177, 0xffff0000, v160\n"
    "v_mul_f32 v176, v250, v176\n"
    "v_mul_f32 v177, v250, v177\n"
    "v_cvt_pk_bf16_f32 v160, v176, v177\n"
    "v_lshlrev_b32 v176, 16, v161\n"
    "v_and_b32 v177, 0xffff0000, v161\n"
    "v_mul_f32 v176, v250, v176\n"
    "v_mul_f32 v177, v250, v177\n"
    "v_cvt_pk_bf16_f32 v161, v176, v177\n"
    "v_lshlrev_b32 v176, 16, v162\n"
    "v_and_b32 v177, 0xffff0000, v162\n"
    "v_mul_f32 v176, v250, v176\n"
    "v_mul_f32 v177, v250, v177\n"
    "v_cvt_pk_bf16_f32 v162, v176, v177\n"
    "v_lshlrev_b32 v176, 16, v163\n"
    "v_and_b32 v177, 0xffff0000, v163\n"
    "v_mul_f32 v176, v250, v176\n"
    "v_mul_f32 v177, v250, v177\n"
    "v_cvt_pk_bf16_f32 v163, v176, v177\n"
    "v_lshlrev_b32 v176, 16, v164\n"
    "v_and_b32 v177, 0xffff0000, v164\n"
    "v_mul_f32 v176, v251, v176\n"
    "v_mul_f32 v177, v251, v177\n"
    "v_cvt_pk_bf16_f32 v164, v176, v177\n"
    "v_lshlrev_b32 v176, 16, v165\n"
    "v_and_b32 v177, 0xffff0000, v165\n"
    "v_mul_f32 v176, v251, v176\n"
    "v_mul_f32 v177, v251, v177\n"
    "v_cvt_pk_bf16_f32 v165, v176, v177\n"
    "v_lshlrev_b32 v176, 16, v166\n"
    "v_and_b32 v177, 0xffff0000, v166\n"
    "v_mul_f32 v176, v251, v176\n"
    "v_mul_f32 v177, v251, v177\n"
    "v_cvt_pk_bf16_f32 v166, v176, v177\n"
    "v_lshlrev_b32 v176, 16, v167\n"
    "v_and_b32 v177, 0xffff0000, v167\n"
    "v_mul_f32 v176, v251, v176\n"
    "v_mul_f32 v177, v251, v177\n"
    "v_cvt_pk_bf16_f32 v167, v176, v177\n"
    ".Lresc_25_%=:\n"
    "v_lshlrev_b32 v176, 16, v168\n"
    "v_and_b32 v177, 0xffff0000, v168\n"
    "v_mul_f32 v176, v250, v176\n"
    "v_mul_f32 v177, v250, v177\n"
    "v_cvt_pk_bf16_f32 v168, v176, v177\n"
    "v_lshlrev_b32 v176, 16, v169\n"
    "v_and_b32 v177, 0xffff0000, v169\n"
    "v_mul_f32 v176, v250, v176\n"
    "v_mul_f32 v177, v250, v177\n"
    "v_cvt_pk_bf16_f32 v169, v176, v177\n"
    "v_lshlrev_b32 v176, 16, v170\n"
    "v_and_b32 v177, 0xffff0000, v170\n"
    "v_mul_f32 v176, v250, v176\n"
    "v_mul_f32 v177, v250, v177\n"
    "v_cvt_pk_bf16_f32 v170, v176, v177\n"
    "v_lshlrev_b32 v176, 16, v171\n"
    "v_and_b32 v177, 0xffff0000, v171\n"
    "v_mul_f32 v176, v250, v176\n"
    "v_mul_f32 v177, v250, v177\n"
    "v_cvt_pk_bf16_f32 v171, v176, v177\n"
    "v_lshlrev_b32 v176, 16, v172\n"
    "v_and_b32 v177, 0xffff0000, v172\n"
    "v_mul_f32 v176, v251, v176\n"
    "v_mul_f32 v177, v251, v177\n"
    "v_cvt_pk_bf16_f32 v172, v176, v177\n"
    "v_lshlrev_b32 v176, 16, v173\n"
    "v_and_b32 v177, 0xffff0000, v173\n"
    "v_mul_f32 v176, v251, v176\n"
    "v_mul_f32 v177, v251, v177\n"
    "v_cvt_pk_bf16_f32 v173, v176, v177\n"
    "v_lshlrev_b32 v176, 16, v174\n"
    "v_and_b32 v177, 0xffff0000, v174\n"
    "v_mul_f32 v176, v251, v176\n"
    "v_mul_f32 v177, v251, v177\n"
    "v_cvt_pk_bf16_f32 v174, v176, v177\n"
    "v_lshlrev_b32 v176, 16, v175\n"
    "v_and_b32 v177, 0xffff0000, v175\n"
    "v_mul_f32 v176, v251, v176\n"
    "v_mul_f32 v177, v251, v177\n"
    "v_cvt_pk_bf16_f32 v175, v176, v177\n"
    ".Lend_%=:\n"
    "s_setpc_b64 s[50:51]\n"
    :
    : "s"(__builtin_amdgcn_kernarg_segment_ptr()), "s"(blk), "s"(wave)
    : "v0", "v1", "v2", "v3", "v4", "v5", "v6", "v7", "v8", "v9", "v10", "v11", "v12", "v13", "v14", "v15", "v16", "v17", "v18", "v19", "v20", "v21", "v22", "v23", "v24", "v25", "v26", "v27", "v28", "v29", "v30", "v31", "v32", "v33", "v34", "v35", "v36", "v37", "v38", "v39", "v40", "v41", "v42", "v43", "v44", "v45", "v46", "v47", "v48", "v49", "v50", "v51", "v52", "v53", "v54", "v55", "v56", "v57", "v58", "v59", "v60", "v61", "v62", "v63", "v64", "v65", "v66", "v67", "v68", "v69", "v70", "v71", "v72", "v73", "v74", "v75", "v76", "v77", "v78", "v79", "v80", "v81", "v82", "v83", "v84", "v85", "v86", "v87", "v88", "v89", "v90", "v91", "v92", "v93", "v94", "v95", "v96", "v97", "v98", "v99", "v100", "v101", "v102", "v103", "v104", "v105", "v106", "v107", "v108", "v109", "v110", "v111", "v112", "v113", "v114", "v115", "v116", "v117", "v118", "v119", "v120", "v121", "v122", "v123", "v124", "v125", "v126", "v127", "v128", "v129", "v130", "v131", "v132", "v133", "v134", "v135", "v136", "v137", "v138", "v139", "v140", "v141", "v142", "v143", "v144", "v145", "v146", "v147", "v148", "v149", "v150", "v151", "v152", "v153", "v154", "v155", "v156", "v157", "v158", "v159", "v160", "v161", "v162", "v163", "v164", "v165", "v166", "v167", "v168", "v169", "v170", "v171", "v172", "v173", "v174", "v175", "v176", "v177", "v178", "v179", "v180", "v181", "v182", "v183", "v184", "v185", "v186", "v187", "v188", "v189", "v190", "v191", "v192", "v193", "v194", "v195", "v196", "v197", "v198", "v199", "v200", "v201", "v202", "v203", "v204", "v205", "v206", "v207", "v208", "v209", "v210", "v211", "v212", "v213", "v214", "v215", "v216", "v217", "v218", "v219", "v220", "v221", "v222", "v223", "v224", "v225", "v226", "v227", "v228", "v229", "v230", "v231", "v232", "v233", "v234", "v235", "v236", "v237", "v238", "v239", "v240", "v241", "v242", "v243", "v244", "v245", "v246", "v247", "v248", "v249", "v250", "v251", "v252", "v253", "v254", "v255",
      "a0", "a1", "a2", "a3", "a4", "a5", "a6", "a7", "a8", "a9", "a10", "a11", "a12", "a13", "a14", "a15", "a16", "a17", "a18", "a19", "a20", "a21", "a22", "a23", "a24", "a25", "a26", "a27", "a28", "a29", "a30", "a31", "a32", "a33", "a34", "a35", "a36", "a37", "a38", "a39", "a40", "a41", "a42", "a43", "a44", "a45", "a46", "a47", "a48", "a49", "a50", "a51", "a52", "a53", "a54", "a55", "a56", "a57", "a58", "a59", "a60", "a61", "a62", "a63", "a64", "a65", "a66", "a67", "a68", "a69", "a70", "a71", "a72", "a73", "a74", "a75", "a76", "a77", "a78", "a79", "a80", "a81", "a82", "a83", "a84", "a85", "a86", "a87", "a88", "a89", "a90", "a91", "a92", "a93", "a94", "a95", "a96", "a97", "a98", "a99", "a100", "a101", "a102", "a103", "a104", "a105", "a106", "a107", "a108", "a109", "a110", "a111", "a112", "a113", "a114", "a115", "a116", "a117", "a118", "a119", "a120", "a121", "a122", "a123", "a124", "a125", "a126", "a127", "a128", "a129", "a130", "a131", "a132", "a133", "a134", "a135", "a136", "a137", "a138", "a139", "a140", "a141", "a142", "a143", "a144", "a145", "a146", "a147", "a148", "a149", "a150", "a151", "a152", "a153", "a154", "a155", "a156", "a157", "a158", "a159", "a160", "a161", "a162", "a163", "a164", "a165", "a166", "a167", "a168", "a169", "a170", "a171", "a172", "a173", "a174", "a175", "a176", "a177", "a178", "a179", "a180", "a181", "a182", "a183", "a184", "a185", "a186", "a187", "a188", "a189", "a190", "a191", "a192", "a193", "a194", "a195", "a196", "a197", "a198", "a199", "a200", "a201", "a202", "a203", "a204", "a205", "a206", "a207", "a208", "a209", "a210", "a211", "a212", "a213", "a214", "a215", "a216", "a217", "a218", "a219", "a220", "a221", "a222", "a223", "a224", "a225", "a226", "a227", "a228", "a229", "a230", "a231", "a232", "a233", "a234", "a235", "a236", "a237", "a238", "a239", "a240", "a241", "a242", "a243", "a244", "a245", "a246", "a247", "a248", "a249", "a250", "a251", "a252", "a253", "a254", "a255",
      "s16", "s17", "s18", "s19", "s20", "s21", "s22", "s23", "s24", "s25", "s26", "s27", "s28", "s29", "s30", "s31", "s36", "s37", "s38", "s39", "s40", "s41", "s42", "s43", "s44", "s45", "s46", "s47", "s48", "s49", "s50", "s51", "s52", "s53", "s54", "s55", "s56", "s57", "s58", "s59", "s60", "s61", "s62", "s63", "s64", "s65", "s66", "s67", "s68", "s69", "s70", "s71", "s72", "s73", "s74", "s75", "s76", "s77", "s78", "s79", "s80", "s81", "s82", "s83", "s84", "s85", "s86", "s87", "s88", "s89", "s90", "s91", "s92", "s93", "s94", "s95", "s96", "s97", "s98", "s99", "s100", "vcc", "scc", "memory");
}


extern "C" void kernel_launch(void* const* d_in, const int* in_sizes, int n_in, void* d_out, int out_size,
                              void* d_ws, size_t ws_size, hipStream_t stream) {
  (void)in_sizes; (void)n_in; (void)out_size; (void)ws_size;
  (void)hipFuncSetAttribute((const void*)gqa_attn_kernel, hipFuncAttributeMaxDynamicSharedMemorySize, 163840);
  gqa_attn_kernel<<<256, 256, 163840, stream>>>(d_in[0], d_in[1], d_in[2], d_out, d_ws);
}
```
